```python
import torch

def _softmax(x, axis=-1):
    return torch.softmax(x, dim=axis)
def _gelu(x):
    return torch.nn.functional.gelu(x, approximate="tanh")
def _silu(x):
    return torch.nn.functional.silu(x)
def _sigmoid(x):
    return torch.sigmoid(x)
def _layernorm(x, axis=-1, eps=1e-5):
    m = x.mean(dim=axis, keepdim=True)
    v = x.var(dim=axis, keepdim=True, unbiased=False)
    return (x - m) / torch.sqrt(v + eps)
def _scatter_add(tgt, idx, src, axis=0):
    return tgt.clone().index_add_(axis, idx, src)
def _segment_sum(src, seg, n):
    out = torch.zeros((n,) + src.shape[1:], dtype=src.dtype)
    return out.index_add_(0, seg, src)
def _pad(x, pw):
    flat = tuple(v for pair in reversed(pw) for v in pair)
    return torch.nn.functional.pad(x, flat)

kernel_name = 'standalone__sliding_block_cached__decode'

def setup_inputs():

    _w = (lambda a: (lambda t: t.to(torch.bfloat16) if t.is_floating_point() else t)(torch.as_tensor(a)))
    B, Sq, D = 2, 1, 640
    Hq, Hkv, Sk, Dh = 32, 32, 4096, 128
    assert Hq % Hkv == 0, (Hq, Hkv)
    return {
        "x": _w((lambda *s: torch.randn(*s, dtype=torch.bfloat16) * 0.8)(B, Sq, D)),
        "k_cache": _w((lambda *s: torch.randn(*s, dtype=torch.bfloat16) * 0.8)(B, Hkv, Sk, Dh)),
        "v_cache": _w((lambda *s: torch.randn(*s, dtype=torch.bfloat16) * 0.05)(B, Hkv, Sk, Dh)),
        "wq": _w((lambda *s: torch.randn(*s, dtype=torch.bfloat16) * 0.8)(D, Hq * Dh) * (D ** -0.5)),
        "wo": _w((lambda *s: torch.randn(*s, dtype=torch.bfloat16) * 0.05)(Hq * Dh, D) * ((Hq * Dh) ** -0.5)),
    }

def reference(x, k_cache, v_cache, wq, wo):
    B, Sq, D = x.shape
    Hq, Dh = 32, 128
    Hkv, Sk = k_cache.shape[1], k_cache.shape[2]
    W = min(4096, max(256, Sk // 4))
    rep = Hq // Hkv
    q = (lambda x, *d: x.permute(*d))((x @ wq).reshape(B, Sq, Hq, Dh), 0, 2, 1, 3)
    k = (lambda x, n, axis: torch.repeat_interleave(x, n, dim=axis))(k_cache[:, :, Sk - W:, :], rep, axis=1)
    v = (lambda x, n, axis: torch.repeat_interleave(x, n, dim=axis))(v_cache[:, :, Sk - W:, :], rep, axis=1)
    scores = torch.einsum("bhqd,bhkd->bhqk", q, k) * (Dh ** -0.5)
    p = _softmax(scores, axis=-1)
    o = torch.einsum("bhqk,bhkd->bhqd", p, v)
    return (lambda x, *d: x.permute(*d))(o, 0, 2, 1, 3).reshape(B, Sq, Hq * Dh) @ wo
```

```cpp
#include <hip/hip_runtime.h>
#include <hip/hip_bf16.h>
#include <cstdint>

typedef unsigned u32x4 __attribute__((ext_vector_type(4)));
typedef float f32x4 __attribute__((ext_vector_type(4)));
typedef __bf16 bf16x2 __attribute__((ext_vector_type(2)));

constexpr int B = 2, D = 640, H = 32, SK = 4096, DH = 128, W = 1024, NS = 4, KPS = W / NS;

__device__ __forceinline__ float bflo(unsigned u) { return __builtin_bit_cast(float, u << 16); }
__device__ __forceinline__ float bfhi(unsigned u) { return __builtin_bit_cast(float, u & 0xffff0000u); }
__device__ __forceinline__ float bf2f(unsigned short u) { return __builtin_bit_cast(float, (unsigned)u << 16); }
__device__ __forceinline__ float rbf(float f) { return (float)(__bf16)f; }

__global__ void __launch_bounds__(256) k_qproj(const unsigned short* x, const unsigned short* wq, float* qpart) {
    __shared__ float red[16][2][128];
    const int h = blockIdx.x >> 3, j = blockIdx.x & 7, tid = threadIdx.x, c = tid & 15, r = tid >> 4;
    float a0[8], a1[8];
#pragma unroll
    for (int k = 0; k < 8; ++k) { a0[k] = 0.f; a1[k] = 0.f; }
#pragma unroll
    for (int i = 0; i < 5; ++i) {
        const int d = j * 80 + i * 16 + r;
        const u32x4 w = *reinterpret_cast<const u32x4*>(wq + (size_t)d * (H * DH) + h * DH + c * 8);
        const float x0 = bf2f(x[d]), x1 = bf2f(x[D + d]);
#pragma unroll
        for (int k = 0; k < 4; ++k) {
            a0[2 * k] += x0 * bflo(w[k]); a0[2 * k + 1] += x0 * bfhi(w[k]);
            a1[2 * k] += x1 * bflo(w[k]); a1[2 * k + 1] += x1 * bfhi(w[k]);
        }
    }
#pragma unroll
    for (int k = 0; k < 8; ++k) { red[r][0][c * 8 + k] = a0[k]; red[r][1][c * 8 + k] = a1[k]; }
    __syncthreads();
    const int b = tid >> 7, n = tid & 127;
    float s = 0.f;
#pragma unroll
    for (int i = 0; i < 16; ++i) s += red[i][b][n];
    qpart[((size_t)j * B + b) * (H * DH) + h * DH + n] = s;
}

__global__ void __launch_bounds__(256) k_attn(const unsigned short* kc, const unsigned short* vc, const float* qpart, float* opart, float* ml) {
    __shared__ float q[128];
    __shared__ float sc[KPS];
    __shared__ float red[16][128];
    __shared__ float wred[8];
    const int s = blockIdx.x & 3, bh = blockIdx.x >> 2, b = bh >> 5, h = bh & 31, tid = threadIdx.x, c = tid & 15, r = tid >> 4;
    if (tid < 128) {
        float a = 0.f;
#pragma unroll
        for (int j = 0; j < 8; ++j) a += qpart[((size_t)j * B + b) * (H * DH) + h * DH + tid];
        q[tid] = rbf(a);
    }
    __syncthreads();
    const size_t base = ((size_t)bh * SK + (SK - W) + s * KPS) * DH;
    float qr[8];
#pragma unroll
    for (int k = 0; k < 8; ++k) qr[k] = q[c * 8 + k];
#pragma unroll
    for (int i = 0; i < 16; ++i) {
        const int key = i * 16 + r;
        const u32x4 w = *reinterpret_cast<const u32x4*>(kc + base + (size_t)key * DH + c * 8);
        float a = 0.f;
#pragma unroll
        for (int k = 0; k < 4; ++k) { a += qr[2 * k] * bflo(w[k]); a += qr[2 * k + 1] * bfhi(w[k]); }
        a += __shfl_xor(a, 1); a += __shfl_xor(a, 2); a += __shfl_xor(a, 4); a += __shfl_xor(a, 8);
        if (c == 0) sc[key] = a * 0.08838834764831845f;
    }
    __syncthreads();
    float v = sc[tid];
    float m = v;
#pragma unroll
    for (int o = 32; o >= 1; o >>= 1) m = fmaxf(m, __shfl_xor(m, o));
    if ((tid & 63) == 0) wred[tid >> 6] = m;
    __syncthreads();
    m = fmaxf(fmaxf(wred[0], wred[1]), fmaxf(wred[2], wred[3]));
    const float p = __expf(v - m);
    float l = p;
#pragma unroll
    for (int o = 32; o >= 1; o >>= 1) l += __shfl_xor(l, o);
    if ((tid & 63) == 0) wred[4 + (tid >> 6)] = l;
    sc[tid] = p;
    __syncthreads();
    l = wred[4] + wred[5] + wred[6] + wred[7];
    float acc[8];
#pragma unroll
    for (int k = 0; k < 8; ++k) acc[k] = 0.f;
#pragma unroll
    for (int i = 0; i < 16; ++i) {
        const int key = i * 16 + r;
        const u32x4 w = *reinterpret_cast<const u32x4*>(vc + base + (size_t)key * DH + c * 8);
        const float pp = sc[key];
#pragma unroll
        for (int k = 0; k < 4; ++k) { acc[2 * k] += pp * bflo(w[k]); acc[2 * k + 1] += pp * bfhi(w[k]); }
    }
#pragma unroll
    for (int k = 0; k < 8; ++k) red[r][c * 8 + k] = acc[k];
    __syncthreads();
    if (tid < 128) {
        float a = 0.f;
#pragma unroll
        for (int i = 0; i < 16; ++i) a += red[i][tid];
        opart[(size_t)blockIdx.x * 128 + tid] = a;
    }
    if (tid == 0) { ml[blockIdx.x * 2] = m; ml[blockIdx.x * 2 + 1] = l; }
}

__global__ void __launch_bounds__(256) k_oproj(const float* opart, const float* ml, const unsigned short* wo, float* ypart) {
    __shared__ float o[2][128];
    __shared__ float red[2][25][80];
    const int h = blockIdx.x >> 3, j = blockIdx.x & 7, tid = threadIdx.x;
    {
        const int b = tid >> 7, n = tid & 127, bh = b * H + h;
        float M = -INFINITY;
#pragma unroll
        for (int s = 0; s < 4; ++s) M = fmaxf(M, ml[(bh * 4 + s) * 2]);
        float L = 0.f, a = 0.f;
#pragma unroll
        for (int s = 0; s < 4; ++s) { const float w = __expf(ml[(bh * 4 + s) * 2] - M); L += w * ml[(bh * 4 + s) * 2 + 1]; a += w * opart[(size_t)(bh * 4 + s) * 128 + n]; }
        o[b][n] = rbf(a / L);
    }
    __syncthreads();
    const int c = tid % 10, r = tid / 10;
    if (r < 25) {
        float a0[8], a1[8];
#pragma unroll
        for (int k = 0; k < 8; ++k) { a0[k] = 0.f; a1[k] = 0.f; }
        for (int row = r; row < 128; row += 25) {
            const u32x4 w = *reinterpret_cast<const u32x4*>(wo + (size_t)(h * DH + row) * D + j * 80 + c * 8);
            const float x0 = o[0][row], x1 = o[1][row];
#pragma unroll
            for (int k = 0; k < 4; ++k) {
                a0[2 * k] += x0 * bflo(w[k]); a0[2 * k + 1] += x0 * bfhi(w[k]);
                a1[2 * k] += x1 * bflo(w[k]); a1[2 * k + 1] += x1 * bfhi(w[k]);
            }
        }
#pragma unroll
        for (int k = 0; k < 8; ++k) { red[0][r][c * 8 + k] = a0[k]; red[1][r][c * 8 + k] = a1[k]; }
    }
    __syncthreads();
    if (tid < 160) {
        const int b = tid / 80, n = tid % 80;
        float a = 0.f;
#pragma unroll
        for (int i = 0; i < 25; ++i) a += red[b][i][n];
        ypart[((size_t)h * B + b) * D + j * 80 + n] = a;
    }
}

__global__ void __launch_bounds__(256) k_final(const float* ypart, __hip_bfloat16* out) {
    const int i = blockIdx.x * 256 + threadIdx.x;
    if (i < B * D) {
        float a = 0.f;
#pragma unroll
        for (int h = 0; h < H; ++h) a += ypart[(size_t)h * B * D + i];
        out[i] = __float2bfloat16(a);
    }
}

extern "C" void kernel_launch(void* const* d_in, const int* in_sizes, int n_in,
                              void* d_out, int out_size, void* d_ws, size_t ws_size,
                              hipStream_t stream) {
    const unsigned short* x = (const unsigned short*)d_in[0];
    const unsigned short* kc = (const unsigned short*)d_in[1];
    const unsigned short* vc = (const unsigned short*)d_in[2];
    const unsigned short* wq = (const unsigned short*)d_in[3];
    const unsigned short* wo = (const unsigned short*)d_in[4];
    float* ws = (float*)d_ws;
    float* qpart = ws;
    float* opart = ws + 65536;
    float* ml = opart + 32768;
    float* ypart = ml + 1024;
    k_qproj<<<256, 256, 0, stream>>>(x, wq, qpart);
    k_attn<<<256, 256, 0, stream>>>(kc, vc, qpart, opart, ml);
    k_oproj<<<256, 256, 0, stream>>>(opart, ml, wo, ypart);
    k_final<<<5, 256, 0, stream>>>(ypart, (__hip_bfloat16*)d_out);
}
```

```cpp
#include <hip/hip_runtime.h>
#include <hip/hip_bf16.h>
#include <cstdint>

typedef unsigned u32;
typedef unsigned long long u64;
typedef u32 u32x4 __attribute__((ext_vector_type(4)));
typedef u32 u32x2 __attribute__((ext_vector_type(2)));
typedef float f32x4 __attribute__((ext_vector_type(4)));
typedef float f32x2 __attribute__((ext_vector_type(2)));
typedef short s16x4 __attribute__((ext_vector_type(4)));
typedef short s16x8 __attribute__((ext_vector_type(8)));
typedef __bf16 bf16x2 __attribute__((ext_vector_type(2)));
typedef __bf16 bf16x8 __attribute__((ext_vector_type(8)));
#define LAS __attribute__((address_space(3)))

#define WIN 6
#define ADA 1
#define ADA_A 310
#define ADA_B 15
#define ADK_A 95
#define ADK_B 14
#define ADA_T 20
#define ADA_HI 9
#define ADA_LO 4
#define MICRO 0
#define X1WARM 1

constexpr int D = 640, H = 32, SK = 4096, DH = 128, W = 1024;
constexpr int NSW = 12, NTHR = (NSW + 1) * 64;

constexpr u32 WS_E = 0;
constexpr u32 WS_XQ = 4096;
constexpr u32 WS_X1 = WS_XQ + 32 * 8 * 1024;
constexpr u32 X1_STRIDE = 640;
constexpr u32 WS_X2 = WS_X1 + 32 * 8 * X1_STRIDE;
constexpr u32 WS_END = WS_X2 + 8 * 5 * 32 * 128;
constexpr u32 WS_STAMP = (WS_END + 4095) & ~4095u;
constexpr u32 SPIN_CAP = 1u << 20;

constexpr int L_QP = 0;
constexpr int L_OP = 4096;
constexpr int L_M = 6144;
constexpr int L_L = 6160;
constexpr int L_Q = 6208;
constexpr int L_O = 6464;
constexpr int L_TAG = 6976;
constexpr int L_FLAG = 6992;
constexpr int L_P = 7120;
constexpr int L_WO = 7632;
constexpr int L_Z = L_WO + 20480;
constexpr int L_END = L_Z + 256;
constexpr int F_QP = 0, F_Q = 1, F_OP = 2, F_O = 3, F_WO = 4, F_VGO = 5  , F_P = 9, F_KDONE = 14, F_KGO = 16  , F_WGO = 20  ;

__device__ __forceinline__ float bflo(u32 u) { return __builtin_bit_cast(float, u << 16); }
__device__ __forceinline__ float bfhi(u32 u) { return __builtin_bit_cast(float, u & 0xffff0000u); }
__device__ __forceinline__ u32 fbits(float f) { return __builtin_bit_cast(u32, f); }
__device__ __forceinline__ float bitsf(u32 u) { return __builtin_bit_cast(float, u); }
__device__ __forceinline__ u32 pack_bf16(float lo, float hi) {
    bf16x2 v = {(__bf16)lo, (__bf16)hi};
    return __builtin_bit_cast(u32, v);
}
__device__ __forceinline__ float dot2(u32 a, u32 b, float acc) {
    return __builtin_amdgcn_fdot2_f32_bf16(__builtin_bit_cast(bf16x2, a), __builtin_bit_cast(bf16x2, b), acc, false);
}
template <int CTRL> __device__ __forceinline__ float dpp(float x) {
    return __builtin_bit_cast(float, __builtin_amdgcn_mov_dpp(__builtin_bit_cast(int, x), CTRL, 0xf, 0xf, true));
}
constexpr int XOR1 = 0xB1, XOR2 = 0x4E, XOR7 = 0x141, ROR8 = 0x128, ROR4 = 0x124;
__device__ __forceinline__ float swap32_add(float a, float b) {
    auto s = __builtin_amdgcn_permlane32_swap(fbits(a), fbits(b), false, false);
    return bitsf(s[0]) + bitsf(s[1]);
}
__device__ __forceinline__ float swap16_add(float a, float b) {
    auto s = __builtin_amdgcn_permlane16_swap(fbits(a), fbits(b), false, false);
    return bitsf(s[0]) + bitsf(s[1]);
}
__device__ __forceinline__ float swap32_max(float a) {
    auto s = __builtin_amdgcn_permlane32_swap(fbits(a), fbits(a), false, false);
    return fmaxf(bitsf(s[0]), bitsf(s[1]));
}
__device__ __forceinline__ float swap16_max(float a) {
    auto s = __builtin_amdgcn_permlane16_swap(fbits(a), fbits(a), false, false);
    return fmaxf(bitsf(s[0]), bitsf(s[1]));
}
__device__ __forceinline__ float trunc24(float v) { return bitsf(fbits(v) & 0xffffff00u); }
__device__ __forceinline__ u32x4 enc4(float a, float b, float c, float d, u32 tagA, u32 tagB) {
    const u32 fa = fbits(a) + 0x80u, fb = fbits(b) + 0x80u, fc = fbits(c) + 0x80u, fd = fbits(d) + 0x80u;
    return u32x4{__builtin_amdgcn_perm(fb, fa, 0x05030201u), __builtin_amdgcn_perm(tagA, fb, 0x05040302u),
                 __builtin_amdgcn_perm(fd, fc, 0x05030201u), __builtin_amdgcn_perm(tagB, fd, 0x05040302u)};
}
__device__ __forceinline__ f32x4 dec4(u32x4 g) {
    return f32x4{bitsf(g[0] << 8), bitsf(__builtin_amdgcn_perm(g[1], g[0], 0x0504030cu)),
                 bitsf(g[2] << 8), bitsf(__builtin_amdgcn_perm(g[3], g[2], 0x0504030cu))};
}
__device__ __forceinline__ u32 badg(u32x4 g, u32 tagA, u32 tagB) { return ((g[1] >> 16) ^ tagA) | ((g[3] >> 16) ^ tagB); }

__device__ __forceinline__ void flag_add(unsigned char* lds, int f, int lane) {
    if (MICRO) asm volatile("" ::: "memory"); else asm volatile("s_waitcnt lgkmcnt(0)" ::: "memory");
    if (lane == 0) __hip_atomic_fetch_add((LAS u32*)(lds + L_FLAG) + f, 1u, __ATOMIC_RELAXED, __HIP_MEMORY_SCOPE_WORKGROUP);
    asm volatile("" ::: "memory");
}
__device__ __forceinline__ u32 flag_add_ret(unsigned char* lds, int f, int lane) {
    if (MICRO) asm volatile("" ::: "memory"); else asm volatile("s_waitcnt lgkmcnt(0)" ::: "memory");
    u32 old = 0;
    if (lane == 0) old = __hip_atomic_fetch_add((LAS u32*)(lds + L_FLAG) + f, 1u, __ATOMIC_RELAXED, __HIP_MEMORY_SCOPE_WORKGROUP);
    old = __builtin_amdgcn_readfirstlane(old);
    asm volatile("" ::: "memory");
    return old;
}
__device__ __forceinline__ u32 flag_get(unsigned char* lds, int f) {
    asm volatile("" ::: "memory");
    const u32 v = __hip_atomic_load((LAS u32*)(lds + L_FLAG) + f, __ATOMIC_RELAXED, __HIP_MEMORY_SCOPE_WORKGROUP);
    return __builtin_amdgcn_readfirstlane(v);
}
__device__ __forceinline__ void flag_wait(unsigned char* lds, int f, u32 want) {
#pragma nounroll
    for (u32 it = 0; it < (1u << 24); ++it) {
        if (flag_get(lds, f) >= want) break;
        __builtin_amdgcn_s_sleep(1);
    }
    asm volatile("" ::: "memory");
}
__device__ __forceinline__ void flag_wait_fast(unsigned char* lds, int f, u32 want) {
#pragma nounroll
    for (u32 it = 0; it < (1u << 26); ++it) {
        if (flag_get(lds, f) >= want) break;
        if (!MICRO) __builtin_amdgcn_s_sleep(1);
    }
    asm volatile("" ::: "memory");
}
template <int N> __device__ __forceinline__ void wait_vm() {
    __builtin_amdgcn_sched_barrier(0);
    asm volatile("s_waitcnt vmcnt(%0)" :: "n"(N) : "memory");
    __builtin_amdgcn_sched_barrier(0);
}
__device__ __forceinline__ void ada_wait(u64 tstart, int target) {
    if (ADA) {
        const int late = (int)(__builtin_amdgcn_s_memrealtime() - tstart) - target;
        if (late > ADA_T) wait_vm<ADA_HI - 1>(); else if (late < -ADA_T) wait_vm<ADA_LO - 1>(); else wait_vm<WIN - 1>();
    } else wait_vm<WIN - 1>();
}
template <int N> __device__ __forceinline__ void release_credits(unsigned char* lds, int f, int lane) {
    if constexpr (N > 0) { wait_vm<N - 1>(); flag_add(lds, f, lane); release_credits<N - 1>(lds, f, lane); }
}

#define STAMP(i) do { } while (0)

__global__ void __launch_bounds__(NTHR) fused_decode(const unsigned short* __restrict__ x, const u32x4* __restrict__ kc,
                                                     const u32x4* __restrict__ vc, const u32x4* __restrict__ wq,
                                                     const u32x4* __restrict__ wo, unsigned short* __restrict__ out,
                                                     unsigned char* __restrict__ ws) {
    __shared__ __attribute__((aligned(16))) unsigned char lds[L_END];
    const int tid = threadIdx.x, lane = tid & 63;
    const int wv = __builtin_amdgcn_readfirstlane(tid >> 6);
    const int g = blockIdx.x & 7, idx = blockIdx.x >> 3;
    const int h = g * 4 + (idx >> 3), j = idx & 7, b = j >> 2, s = j & 3;
    const __amdgpu_buffer_rsrc_t wsr = __builtin_amdgcn_make_buffer_rsrc(ws, 0, WS_END, 0x00020000);
    asm volatile("" :: "s"(x), "s"(kc), "s"(vc), "s"(wq), "s"(wo), "s"(out), "s"(ws));
    STAMP(0);
    const u64 tstart = __builtin_amdgcn_s_memrealtime();
    const int r = lane >> 4, c = lane & 15;
    const size_t kvbase = ((size_t)(b * H + h) * SK + (SK - W) + s * 256 + (wv & 3) * 64) * (DH / 8) + lane;

    if (wv < 4) {
        u32x4 kv[16];
#pragma unroll
        for (int i = 0; i < WIN - 5; ++i) kv[i] = __builtin_nontemporal_load(kc + kvbase + i * 64);
        __builtin_amdgcn_sched_barrier(0);
        __builtin_amdgcn_s_barrier();
        asm volatile("" ::: "memory");
#pragma unroll
        for (int i = WIN - 5; i < WIN; ++i) { flag_wait(lds, F_KGO + wv, i - (WIN - 5) + 1); kv[i] = __builtin_nontemporal_load(kc + kvbase + i * 64); __builtin_amdgcn_sched_barrier(0); }
        STAMP(1);
#pragma unroll
        for (int i = WIN; i < 16; ++i) { ada_wait(tstart, ADK_A + ADK_B * i); kv[i] = __builtin_nontemporal_load(kc + kvbase + i * 64); }
        __builtin_amdgcn_sched_barrier(0);
        flag_add(lds, F_KDONE, lane);
        release_credits<WIN>(lds, F_VGO + wv, lane);
        STAMP(2);
        flag_wait(lds, F_Q, 1);
        STAMP(3);
        const u32x4 q4 = *reinterpret_cast<const u32x4*>(lds + L_Q + c * 16);
        float xs[16];
#pragma unroll
        for (int i = 0; i < 16; ++i) {
            float d = 0.f;
#pragma unroll
            for (int k = 0; k < 4; ++k) d = dot2(kv[i][k], q4[k], d);
            xs[i] = d;
        }
#define TR_STEP(HALF, CTRL, BIT) _Pragma("unroll") for (int t_ = 0; t_ < (HALF); ++t_) { \
            const float lo_ = xs[t_], hi_ = xs[t_ + (HALF)]; const float t1_ = lo_ + dpp<CTRL>(lo_), t2_ = hi_ + dpp<CTRL>(hi_); \
            xs[t_] = (c & (BIT)) ? t2_ : t1_; }
        TR_STEP(8, ROR8, 8) TR_STEP(4, XOR7, 4) TR_STEP(2, XOR2, 2) TR_STEP(1, XOR1, 1)
#undef TR_STEP
        constexpr float C2 = 0.08838834764831845f * 1.4426950408889634f;
        const float sc = xs[0] * C2;
        float m = fmaxf(sc, dpp<XOR1>(sc));
        m = fmaxf(m, dpp<XOR2>(m));
        m = fmaxf(m, dpp<ROR4>(m)); m = fmaxf(m, dpp<ROR8>(m));
        m = swap16_max(m); m = swap32_max(m);
        const float p = __builtin_amdgcn_exp2f(sc - m);
        float l = p + dpp<XOR1>(p);
        l += dpp<XOR2>(l);
        l += dpp<ROR4>(l); l += dpp<ROR8>(l);
        l = swap16_add(l, l); l = swap32_add(l, l);
        const u32 ppk = pack_bf16(p, dpp<XOR1>(p));
        if ((c & 1) == 0) *reinterpret_cast<u32*>(lds + L_P + wv * 128 + r * 32 + (c >> 1) * 4) = ppk;
        if (lane == 0) { *reinterpret_cast<float*>(lds + L_M + wv * 4) = m; *reinterpret_cast<float*>(lds + L_L + wv * 4) = l; }
        flag_add(lds, F_P, lane);
        STAMP(4);
    } else if (wv < 8) {
        const int v = wv - 4;
        u32x4 vv[16];
        __builtin_amdgcn_s_barrier();
        asm volatile("" ::: "memory");
#pragma unroll
        for (int i = 0; i < WIN; ++i) { flag_wait(lds, F_VGO + v, i + 1); vv[i] = __builtin_nontemporal_load(vc + kvbase + i * 64); __builtin_amdgcn_sched_barrier(0); }
        STAMP(1);
#pragma unroll
        for (int i = WIN; i < 16; ++i) { ada_wait(tstart, ADA_A + ADA_B * i); vv[i] = __builtin_nontemporal_load(vc + kvbase + i * 64); }
        __builtin_amdgcn_sched_barrier(0);
        flag_add(lds, F_WGO + v, lane);
        STAMP(2);
#define VPERM(i_) _Pragma("unroll") for (int k = 0; k < 4; ++k) { \
            const u32 lo = __builtin_amdgcn_perm(vv[2 * (i_) + 1][k], vv[2 * (i_)][k], 0x05040100u); \
            const u32 hi = __builtin_amdgcn_perm(vv[2 * (i_) + 1][k], vv[2 * (i_)][k], 0x07060302u); \
            vv[2 * (i_)][k] = lo; vv[2 * (i_) + 1][k] = hi; }
#define VDOT(i_) { const u32 pp = (i_) < 4 ? pa[(i_) & 3] : pb[(i_) & 3]; _Pragma("unroll") for (int k = 0; k < 4; ++k) { \
            acc[2 * k] = dot2(vv[2 * (i_)][k], pp, acc[2 * k]); acc[2 * k + 1] = dot2(vv[2 * (i_) + 1][k], pp, acc[2 * k + 1]); } }
        VPERM(0) VPERM(1) VPERM(2) VPERM(3) VPERM(4)
#pragma unroll
        for (int i = 0; i < 10; ++i) asm volatile("" : "+v"(vv[i]));
        __builtin_amdgcn_sched_barrier(0);
        flag_wait(lds, F_P, 4);
        STAMP(3);
        const u32x4 pa = *reinterpret_cast<const u32x4*>(lds + L_P + v * 128 + r * 32);
        const u32x4 pb = *reinterpret_cast<const u32x4*>(lds + L_P + v * 128 + r * 32 + 16);
        const u32x2 tg = *reinterpret_cast<const u32x2*>(lds + L_TAG);
        const f32x4 m4 = *reinterpret_cast<const f32x4*>(lds + L_M);
        const float mown = *reinterpret_cast<const float*>(lds + L_M + v * 4);
        const float M = fmaxf(fmaxf(m4[0], m4[1]), fmaxf(m4[2], m4[3]));
        const float fv = __builtin_amdgcn_exp2f(mown - M);
        float acc[8];
#pragma unroll
        for (int k = 0; k < 8; ++k) acc[k] = 0.f;
        VDOT(0) VDOT(1) VDOT(2) VDOT(3) VDOT(4)
        __builtin_amdgcn_sched_barrier(0);
        VPERM(5) VDOT(5)
        __builtin_amdgcn_sched_barrier(0);
        VPERM(6) VDOT(6)
        __builtin_amdgcn_sched_barrier(0);
        VPERM(7) VDOT(7)
#undef VPERM
#undef VDOT
        {
            float u[4];
#pragma unroll
            for (int k = 0; k < 4; ++k) u[k] = swap32_add(acc[k], acc[k + 4]);
            f32x2 t;
            t[0] = swap16_add(u[0], u[2]) * fv; t[1] = swap16_add(u[1], u[3]) * fv;
            *reinterpret_cast<f32x2*>(lds + L_OP + v * 512 + (8 * c + 2 * r) * 4) = t;
        }
        STAMP(4);
        if (flag_add_ret(lds, F_OP, lane) == 3) {
            const int l5 = lane & 31, hf = lane >> 5;
            const f32x4 a0 = *reinterpret_cast<const f32x4*>(lds + L_OP + (2 * hf) * 512 + l5 * 16);
            const f32x4 a1 = *reinterpret_cast<const f32x4*>(lds + L_OP + (2 * hf + 1) * 512 + l5 * 16);
            const f32x4 a = a0 + a1;
            float o[4];
#pragma unroll
            for (int k = 0; k < 4; ++k) o[k] = swap32_add(a[k], a[k]);
            if (lane < 32) __builtin_amdgcn_raw_buffer_store_b128(enc4(o[0], o[1], o[2], o[3], tg[0], tg[1]), wsr,
                                                                  WS_X1 + (u32)(h * 8 + j) * X1_STRIDE + lane * 16, 0, 16);
            STAMP(5);
        }
    } else if (wv < NSW) {
        const int w4 = wv - 8;
        u32x4 wqv[5];
        const int xl = lane < 40 ? lane : 0;
        const u32x2 xv = *reinterpret_cast<const u32x2*>(x + (xl >= 20 ? D + (xl - 20) * 4 : xl * 4) + j * 80);
        __builtin_amdgcn_sched_barrier(0);
        {
            const u32x4* p = wq + (size_t)(j * 80 + w4 * 4 + r) * (H * DH / 8) + h * (DH / 8) + c;
#pragma unroll
            for (int i = 0; i < 5; ++i) wqv[i] = __builtin_nontemporal_load(p + (size_t)i * 16 * (H * DH / 8));
        }
        __builtin_amdgcn_sched_barrier(0);
        __builtin_amdgcn_s_barrier();
        asm volatile("" ::: "memory");
        release_credits<5>(lds, F_KGO + w4, lane);
        {
            float a0[8], a1[8];
#pragma unroll
            for (int k = 0; k < 8; ++k) { a0[k] = 0.f; a1[k] = 0.f; }
#pragma unroll
            for (int i = 0; i < 5; ++i) {
                const u32 xa0 = __builtin_amdgcn_readlane(xv[0], i * 4 + w4), xa1 = __builtin_amdgcn_readlane(xv[1], i * 4 + w4);
                const u32 xb0 = __builtin_amdgcn_readlane(xv[0], 20 + i * 4 + w4), xb1 = __builtin_amdgcn_readlane(xv[1], 20 + i * 4 + w4);
                const u32 sa = (r & 2) ? xa1 : xa0, sb = (r & 2) ? xb1 : xb0;
                const float x0 = (r & 1) ? bfhi(sa) : bflo(sa), x1 = (r & 1) ? bfhi(sb) : bflo(sb);
#pragma unroll
                for (int k = 0; k < 4; ++k) {
                    const float lo = bflo(wqv[i][k]), hi = bfhi(wqv[i][k]);
                    a0[2 * k] = fmaf(x0, lo, a0[2 * k]); a0[2 * k + 1] = fmaf(x0, hi, a0[2 * k + 1]);
                    a1[2 * k] = fmaf(x1, lo, a1[2 * k]); a1[2 * k + 1] = fmaf(x1, hi, a1[2 * k + 1]);
                }
            }
            float u[8];
#pragma unroll
            for (int k = 0; k < 8; ++k) u[k] = swap32_add(a0[k], a1[k]);
            f32x4 t;
#pragma unroll
            for (int k = 0; k < 4; ++k) t[k] = swap16_add(u[k], u[k + 4]);
            *reinterpret_cast<f32x4*>(lds + L_QP + w4 * 1024 + ((r >> 1) * 128 + 8 * c + 4 * (r & 1)) * 4) = t;
        }
        flag_add(lds, F_QP, lane);
        STAMP(1);
        const int t2 = tid - 512;
        const u32x4* wop[5];
#pragma unroll
        for (int i = 0; i < 5; ++i) {
            const u32 id = i * 256 + t2, row = id / 10, cc = id - row * 10;
            wop[i] = wo + (size_t)(h * DH + row) * (D / 8) + j * 10 + cc;
        }
        flag_wait(lds, F_WGO + w4, 1);
        u32x4 wov[5];
#pragma unroll
        for (int i = 0; i < 5; ++i) wov[i] = __builtin_nontemporal_load(wop[i]);
        __builtin_amdgcn_sched_barrier(0);
        STAMP(2);
#pragma unroll
        for (int i = 0; i < 5; ++i) *reinterpret_cast<u32x4*>(lds + L_WO + (i * 256 + t2) * 16) = wov[i];
        flag_add(lds, F_WO, lane);
        STAMP(3);
    } else {
        const u64 E = __hip_atomic_load(reinterpret_cast<u64*>(ws + WS_E), __ATOMIC_RELAXED, __HIP_MEMORY_SCOPE_AGENT);
        if (lane < 32) *reinterpret_cast<u32*>(lds + L_FLAG + lane * 4) = 0u;
        *reinterpret_cast<u32*>(lds + L_Z + lane * 4) = 0u;
        asm volatile("s_waitcnt lgkmcnt(0)" ::: "memory");
        __builtin_amdgcn_s_barrier();
        asm volatile("" ::: "memory");
        const u64 T = E + 1;
        const u32 T32 = __builtin_amdgcn_readfirstlane((u32)T);
        const u32 tagA = T32 & 0xffffu, tagB = ((T32 >> 16) + T32 * 40503u) & 0xffffu;
        if (lane == 0) *reinterpret_cast<u32x2*>(lds + L_TAG) = u32x2{tagA, tagB};
        const int l5 = lane & 31, hf = lane >> 5;
        flag_wait(lds, F_QP, 4);
        STAMP(1);
        {
            f32x4 a = *reinterpret_cast<const f32x4*>(lds + L_QP + lane * 16);
#pragma unroll
            for (int w = 1; w < 4; ++w) a += *reinterpret_cast<const f32x4*>(lds + L_QP + w * 1024 + lane * 16);
            __builtin_amdgcn_raw_buffer_store_b128(enc4(a[0], a[1], a[2], a[3], tagA, tagB), wsr, WS_XQ + (u32)(h * 8 + j) * 1024 + lane * 16, 0, 16);
        }
        u32 spins = 0;
        flag_wait(lds, F_KDONE, 4);
        {
            const u32 off = WS_XQ + (u32)(h * 8 + hf) * 1024 + b * 512 + l5 * 16;
            u32x4 gq[4];
            for (;;) {
                asm volatile("" ::: "memory");
                u32 bad = 0;
#pragma unroll
                for (int t = 0; t < 4; ++t) gq[t] = __builtin_amdgcn_raw_buffer_load_b128(wsr, off + t * 2048, 0, 16);
#pragma unroll
                for (int t = 0; t < 4; ++t) bad |= badg(gq[t], tagA, tagB);
                if (!__any(bad != 0) || ++spins > SPIN_CAP) break;
            }
            const f32x4 qs = (dec4(gq[0]) + dec4(gq[1])) + (dec4(gq[2]) + dec4(gq[3]));
            const float q0 = swap32_add(qs[0], qs[2]), q1 = swap32_add(qs[1], qs[3]);
            *reinterpret_cast<u32*>(lds + L_Q + l5 * 8 + hf * 4) = pack_bf16(q0, q1);
            flag_add(lds, F_Q, lane);
        }
        STAMP(2);
        if (X1WARM) {
            const u32x4 wrm = __builtin_amdgcn_raw_buffer_load_b128(wsr, WS_X1 + (u32)(h * 8 + j) * X1_STRIDE + l5 * 16, 0, 16);
            asm volatile("" :: "v"(wrm));
        }
        flag_wait(lds, F_P, 4);
        {
            const f32x4 m4 = *reinterpret_cast<const f32x4*>(lds + L_M);
            const f32x4 l4 = *reinterpret_cast<const f32x4*>(lds + L_L);
            const float M = fmaxf(fmaxf(m4[0], m4[1]), fmaxf(m4[2], m4[3]));
            const float Mhi = trunc24(M), Mlo = M - Mhi;
            float L = 0.f;
#pragma unroll
            for (int w = 0; w < 4; ++w) L = fmaf(__builtin_amdgcn_exp2f(m4[w] - M), l4[w], L);
            if (lane < 8) __builtin_amdgcn_raw_buffer_store_b128(enc4(Mhi, L, Mlo, L, tagA, tagB), wsr,
                                                                 WS_X1 + (u32)(h * 8 + j) * X1_STRIDE + 512 + lane * 16, 0, 16);
        }
        STAMP(3);
        flag_wait_fast(lds, F_OP, 4);
        STAMP(4);
        {
            const u32 off = WS_X1 + (u32)(h * 8) * X1_STRIDE;
            u32x4 go[4], gm;
            for (;;) {
                asm volatile("" ::: "memory");
                gm = __builtin_amdgcn_raw_buffer_load_b128(wsr, off + (lane & 7) * X1_STRIDE + 512, 0, 16);
#pragma unroll
                for (int t = 0; t < 4; ++t) go[t] = __builtin_amdgcn_raw_buffer_load_b128(wsr, off + (2 * t + hf) * X1_STRIDE + l5 * 16, 0, 16);
                u32 bad = badg(gm, tagA, tagB);
#pragma unroll
                for (int t = 0; t < 4; ++t) bad |= badg(go[t], tagA, tagB);
                if (!__any(bad != 0) || ++spins > SPIN_CAP) break;
            }
            const f32x4 ml = dec4(gm);
            const float ms = ml[0] + ml[2];
            float M = fmaxf(ms, dpp<XOR1>(ms)); M = fmaxf(M, dpp<XOR2>(M));
            const float f = __builtin_amdgcn_exp2f(ms - M);
            float L = f * ml[1]; L += dpp<XOR1>(L); L += dpp<XOR2>(L);
            const float fw = f * __builtin_amdgcn_rcpf(L);
            float wt[4];
#pragma unroll
            for (int t = 0; t < 4; ++t) {
                const float w0 = bitsf(__builtin_amdgcn_readlane(fbits(fw), 2 * t)), w1 = bitsf(__builtin_amdgcn_readlane(fbits(fw), 2 * t + 1));
                wt[t] = hf ? w1 : w0;
            }
            const f32x4 ob0 = dec4(go[0]) * wt[0] + dec4(go[1]) * wt[1];
            const f32x4 ob1 = dec4(go[2]) * wt[2] + dec4(go[3]) * wt[3];
            float o[4];
#pragma unroll
            for (int k = 0; k < 4; ++k) o[k] = swap32_add(ob0[k], ob1[k]);
            *reinterpret_cast<u32x2*>(lds + L_O + lane * 8) = u32x2{pack_bf16(o[0], o[1]), pack_bf16(o[2], o[3])};
            flag_add(lds, F_O, lane);
        }
        STAMP(5);
        const int rho = h / 6;
        if (h == rho * 6 && rho < 5) {
            const u32 off = WS_X2 + (u32)((j * 5 + rho) * 32) * 128 + lane * 16;
            u32x4 g0, g1, g2, g3;
            for (;;) {
                asm volatile("" ::: "memory");
                g0 = __builtin_amdgcn_raw_buffer_load_b128(wsr, off, 0, 16);
                g1 = __builtin_amdgcn_raw_buffer_load_b128(wsr, off + 1024, 0, 16);
                g2 = __builtin_amdgcn_raw_buffer_load_b128(wsr, off + 2048, 0, 16);
                g3 = __builtin_amdgcn_raw_buffer_load_b128(wsr, off + 3072, 0, 16);
                const u32 bad = badg(g0, tagA, tagB) | badg(g1, tagA, tagB) | badg(g2, tagA, tagB) | badg(g3, tagA, tagB);
                if (!__any(bad != 0)) break;
                if (++spins > SPIN_CAP) break;
            }
            STAMP(6);
            const f32x4 y4 = (dec4(g0) + dec4(g1)) + (dec4(g2) + dec4(g3));
            const float ya = swap32_add(y4[0], y4[1]), yb = swap32_add(y4[2], y4[3]);
            float y = swap16_add(ya, yb);
            y += dpp<ROR8>(y);
            if ((lane & 8) == 0) {
                const __bf16 ybf = (__bf16)y;
                out[hf * D + j * 80 + 16 * rho + 2 * (lane & 7) + ((lane >> 4) & 1)] = __builtin_bit_cast(unsigned short, ybf);
            }
            if (blockIdx.x == 0 && lane == 0) __hip_atomic_store(reinterpret_cast<u64*>(ws + WS_E), T, __ATOMIC_RELAXED, __HIP_MEMORY_SCOPE_AGENT);
        }
        STAMP(7);
    }
    if (wv < 4 || wv == 8) {
        const int nt = wv < 4 ? wv : 4;
        flag_wait(lds, F_WO, 4);
        const int q_ = (lane & 15) >> 2, p_ = lane & 3;
        const u32 tb = L_WO + (u32)(8 * r + q_) * 160 + 32 * nt + 8 * p_;
        s16x8 bf[4];
#pragma unroll
        for (int t = 0; t < 4; ++t) {
            const s16x4 b0 = __builtin_amdgcn_ds_read_tr16_b64_v4i16((LAS s16x4*)(lds + tb + t * 32 * 160));
            const s16x4 b1 = __builtin_amdgcn_ds_read_tr16_b64_v4i16((LAS s16x4*)(lds + tb + t * 32 * 160 + 4 * 160));
            bf[t] = s16x8{b0[0], b0[1], b0[2], b0[3], b1[0], b1[1], b1[2], b1[3]};
        }
        const u32x2 tg = *reinterpret_cast<const u32x2*>(lds + L_TAG);
        asm volatile("" : "+v"(bf[0]), "+v"(bf[1]), "+v"(bf[2]), "+v"(bf[3]));
        const u32 ab = (c < 2 ? L_O + c * 256 : L_Z) + 16 * r;
        flag_wait_fast(lds, F_O, 1);
        STAMP(6);
        f32x4 dacc = {0.f, 0.f, 0.f, 0.f}, dacc2 = {0.f, 0.f, 0.f, 0.f};
        u32x4 af[4];
#pragma unroll
        for (int t = 0; t < 4; ++t) af[t] = *reinterpret_cast<const u32x4*>(lds + ab + 64 * t);
        dacc = __builtin_amdgcn_mfma_f32_16x16x32_bf16(__builtin_bit_cast(bf16x8, af[0]), __builtin_bit_cast(bf16x8, bf[0]), dacc, 0, 0, 0);
        dacc2 = __builtin_amdgcn_mfma_f32_16x16x32_bf16(__builtin_bit_cast(bf16x8, af[1]), __builtin_bit_cast(bf16x8, bf[1]), dacc2, 0, 0, 0);
        dacc = __builtin_amdgcn_mfma_f32_16x16x32_bf16(__builtin_bit_cast(bf16x8, af[2]), __builtin_bit_cast(bf16x8, bf[2]), dacc, 0, 0, 0);
        dacc2 = __builtin_amdgcn_mfma_f32_16x16x32_bf16(__builtin_bit_cast(bf16x8, af[3]), __builtin_bit_cast(bf16x8, bf[3]), dacc2, 0, 0, 0);
        dacc += dacc2;
        const float n0 = dpp<XOR1>(dacc[0]), n1 = dpp<XOR1>(dacc[1]);
        if (lane < 16 && (lane & 1) == 0) {
            __builtin_amdgcn_raw_buffer_store_b128(enc4(dacc[0], dacc[1], n0, n1, tg[0], tg[1]), wsr,
                                                   WS_X2 + (u32)((j * 5 + nt) * 32 + h) * 128 + (lane >> 1) * 16, 0, 16);
        }
        STAMP(7);
    }
}

extern "C" void kernel_launch(void* const* d_in, const int* in_sizes, int n_in,
                              void* d_out, int out_size, void* d_ws, size_t ws_size,
                              hipStream_t stream) {
    fused_decode<<<256, NTHR, 0, stream>>>((const unsigned short*)d_in[0], (const u32x4*)d_in[1], (const u32x4*)d_in[2],
                                           (const u32x4*)d_in[3], (const u32x4*)d_in[4], (unsigned short*)d_out,
                                           (unsigned char*)d_ws);
}
```

```cpp
#define PROBE_W 12
#define PROBE_A 0
#define PROBE_B 7
#include <hip/hip_runtime.h>
#include <hip/hip_bf16.h>
#include <cstdint>

typedef unsigned u32;
typedef unsigned long long u64;
typedef u32 u32x4 __attribute__((ext_vector_type(4)));
typedef u32 u32x2 __attribute__((ext_vector_type(2)));
typedef float f32x4 __attribute__((ext_vector_type(4)));
typedef float f32x2 __attribute__((ext_vector_type(2)));
typedef short s16x4 __attribute__((ext_vector_type(4)));
typedef short s16x8 __attribute__((ext_vector_type(8)));
typedef __bf16 bf16x2 __attribute__((ext_vector_type(2)));
typedef __bf16 bf16x8 __attribute__((ext_vector_type(8)));
#define LAS __attribute__((address_space(3)))

#define WIN 6
#define ADA 1
#define ADA_A 310
#define ADA_B 15
#define ADK_A 95
#define ADK_B 14
#define ADA_T 20
#define ADA_HI 9
#define ADA_LO 4
#define MICRO 0
#define X1WARM 1

constexpr int D = 640, H = 32, SK = 4096, DH = 128, W = 1024;
constexpr int NSW = 12, NTHR = (NSW + 1) * 64;

constexpr u32 WS_E = 0;
constexpr u32 WS_XQ = 4096;
constexpr u32 WS_X1 = WS_XQ + 32 * 8 * 1024;
constexpr u32 X1_STRIDE = 640;
constexpr u32 WS_X2 = WS_X1 + 32 * 8 * X1_STRIDE;
constexpr u32 WS_END = WS_X2 + 8 * 5 * 32 * 128;
constexpr u32 WS_STAMP = (WS_END + 4095) & ~4095u;
constexpr u32 SPIN_CAP = 1u << 20;

constexpr int L_QP = 0;
constexpr int L_OP = 4096;
constexpr int L_M = 6144;
constexpr int L_L = 6160;
constexpr int L_Q = 6208;
constexpr int L_O = 6464;
constexpr int L_TAG = 6976;
constexpr int L_FLAG = 6992;
constexpr int L_P = 7120;
constexpr int L_WO = 7632;
constexpr int L_Z = L_WO + 20480;
constexpr int L_END = L_Z + 256;
constexpr int F_QP = 0, F_Q = 1, F_OP = 2, F_O = 3, F_WO = 4, F_VGO = 5  , F_P = 9, F_KDONE = 14, F_KGO = 16  , F_WGO = 20  ;

__device__ __forceinline__ float bflo(u32 u) { return __builtin_bit_cast(float, u << 16); }
__device__ __forceinline__ float bfhi(u32 u) { return __builtin_bit_cast(float, u & 0xffff0000u); }
__device__ __forceinline__ u32 fbits(float f) { return __builtin_bit_cast(u32, f); }
__device__ __forceinline__ float bitsf(u32 u) { return __builtin_bit_cast(float, u); }
__device__ __forceinline__ u32 pack_bf16(float lo, float hi) {
    bf16x2 v = {(__bf16)lo, (__bf16)hi};
    return __builtin_bit_cast(u32, v);
}
__device__ __forceinline__ float dot2(u32 a, u32 b, float acc) {
    return __builtin_amdgcn_fdot2_f32_bf16(__builtin_bit_cast(bf16x2, a), __builtin_bit_cast(bf16x2, b), acc, false);
}
template <int CTRL> __device__ __forceinline__ float dpp(float x) {
    return __builtin_bit_cast(float, __builtin_amdgcn_mov_dpp(__builtin_bit_cast(int, x), CTRL, 0xf, 0xf, true));
}
constexpr int XOR1 = 0xB1, XOR2 = 0x4E, XOR7 = 0x141, ROR8 = 0x128, ROR4 = 0x124;
__device__ __forceinline__ float swap32_add(float a, float b) {
    auto s = __builtin_amdgcn_permlane32_swap(fbits(a), fbits(b), false, false);
    return bitsf(s[0]) + bitsf(s[1]);
}
__device__ __forceinline__ float swap16_add(float a, float b) {
    auto s = __builtin_amdgcn_permlane16_swap(fbits(a), fbits(b), false, false);
    return bitsf(s[0]) + bitsf(s[1]);
}
__device__ __forceinline__ float swap32_max(float a) {
    auto s = __builtin_amdgcn_permlane32_swap(fbits(a), fbits(a), false, false);
    return fmaxf(bitsf(s[0]), bitsf(s[1]));
}
__device__ __forceinline__ float swap16_max(float a) {
    auto s = __builtin_amdgcn_permlane16_swap(fbits(a), fbits(a), false, false);
    return fmaxf(bitsf(s[0]), bitsf(s[1]));
}
__device__ __forceinline__ float trunc24(float v) { return bitsf(fbits(v) & 0xffffff00u); }
__device__ __forceinline__ u32x4 enc4(float a, float b, float c, float d, u32 tagA, u32 tagB) {
    const u32 fa = fbits(a) + 0x80u, fb = fbits(b) + 0x80u, fc = fbits(c) + 0x80u, fd = fbits(d) + 0x80u;
    return u32x4{__builtin_amdgcn_perm(fb, fa, 0x05030201u), __builtin_amdgcn_perm(tagA, fb, 0x05040302u),
                 __builtin_amdgcn_perm(fd, fc, 0x05030201u), __builtin_amdgcn_perm(tagB, fd, 0x05040302u)};
}
__device__ __forceinline__ f32x4 dec4(u32x4 g) {
    return f32x4{bitsf(g[0] << 8), bitsf(__builtin_amdgcn_perm(g[1], g[0], 0x0504030cu)),
                 bitsf(g[2] << 8), bitsf(__builtin_amdgcn_perm(g[3], g[2], 0x0504030cu))};
}
__device__ __forceinline__ u32 badg(u32x4 g, u32 tagA, u32 tagB) { return ((g[1] >> 16) ^ tagA) | ((g[3] >> 16) ^ tagB); }

__device__ __forceinline__ void flag_add(unsigned char* lds, int f, int lane) {
    if (MICRO) asm volatile("" ::: "memory"); else asm volatile("s_waitcnt lgkmcnt(0)" ::: "memory");
    if (lane == 0) __hip_atomic_fetch_add((LAS u32*)(lds + L_FLAG) + f, 1u, __ATOMIC_RELAXED, __HIP_MEMORY_SCOPE_WORKGROUP);
    asm volatile("" ::: "memory");
}
__device__ __forceinline__ u32 flag_add_ret(unsigned char* lds, int f, int lane) {
    if (MICRO) asm volatile("" ::: "memory"); else asm volatile("s_waitcnt lgkmcnt(0)" ::: "memory");
    u32 old = 0;
    if (lane == 0) old = __hip_atomic_fetch_add((LAS u32*)(lds + L_FLAG) + f, 1u, __ATOMIC_RELAXED, __HIP_MEMORY_SCOPE_WORKGROUP);
    old = __builtin_amdgcn_readfirstlane(old);
    asm volatile("" ::: "memory");
    return old;
}
__device__ __forceinline__ u32 flag_get(unsigned char* lds, int f) {
    asm volatile("" ::: "memory");
    const u32 v = __hip_atomic_load((LAS u32*)(lds + L_FLAG) + f, __ATOMIC_RELAXED, __HIP_MEMORY_SCOPE_WORKGROUP);
    return __builtin_amdgcn_readfirstlane(v);
}
__device__ __forceinline__ void flag_wait(unsigned char* lds, int f, u32 want) {
#pragma nounroll
    for (u32 it = 0; it < (1u << 24); ++it) {
        if (flag_get(lds, f) >= want) break;
        __builtin_amdgcn_s_sleep(1);
    }
    asm volatile("" ::: "memory");
}
__device__ __forceinline__ void flag_wait_fast(unsigned char* lds, int f, u32 want) {
#pragma nounroll
    for (u32 it = 0; it < (1u << 26); ++it) {
        if (flag_get(lds, f) >= want) break;
        if (!MICRO) __builtin_amdgcn_s_sleep(1);
    }
    asm volatile("" ::: "memory");
}
template <int N> __device__ __forceinline__ void wait_vm() {
    __builtin_amdgcn_sched_barrier(0);
    asm volatile("s_waitcnt vmcnt(%0)" :: "n"(N) : "memory");
    __builtin_amdgcn_sched_barrier(0);
}
__device__ __forceinline__ void ada_wait(u64 tstart, int target) {
    if (ADA) {
        const int late = (int)(__builtin_amdgcn_s_memrealtime() - tstart) - target;
        if (late > ADA_T) wait_vm<ADA_HI - 1>(); else if (late < -ADA_T) wait_vm<ADA_LO - 1>(); else wait_vm<WIN - 1>();
    } else wait_vm<WIN - 1>();
}
template <int N> __device__ __forceinline__ void release_credits(unsigned char* lds, int f, int lane) {
    if constexpr (N > 0) { wait_vm<N - 1>(); flag_add(lds, f, lane); release_credits<N - 1>(lds, f, lane); }
}

#define STAMP(i) do { if ((i) == PROBE_A || (i) == PROBE_B) st[i] = __builtin_amdgcn_s_memrealtime(); } while (0)

__global__ void __launch_bounds__(NTHR) fused_decode(const unsigned short* __restrict__ x, const u32x4* __restrict__ kc,
                                                     const u32x4* __restrict__ vc, const u32x4* __restrict__ wq,
                                                     const u32x4* __restrict__ wo, unsigned short* __restrict__ out,
                                                     unsigned char* __restrict__ ws) {
    __shared__ __attribute__((aligned(16))) unsigned char lds[L_END];
    const int tid = threadIdx.x, lane = tid & 63;
    const int wv = __builtin_amdgcn_readfirstlane(tid >> 6);
    const int g = blockIdx.x & 7, idx = blockIdx.x >> 3;
    const int h = g * 4 + (idx >> 3), j = idx & 7, b = j >> 2, s = j & 3;
    const __amdgpu_buffer_rsrc_t wsr = __builtin_amdgcn_make_buffer_rsrc(ws, 0, WS_END, 0x00020000);
    asm volatile("" :: "s"(x), "s"(kc), "s"(vc), "s"(wq), "s"(wo), "s"(out), "s"(ws));
    u64 st[8] = {0, 0, 0, 0, 0, 0, 0, 0};
    STAMP(0);
    const u64 tstart = __builtin_amdgcn_s_memrealtime();
    const int r = lane >> 4, c = lane & 15;
    const size_t kvbase = ((size_t)(b * H + h) * SK + (SK - W) + s * 256 + (wv & 3) * 64) * (DH / 8) + lane;

    if (wv < 4) {
        u32x4 kv[16];
#pragma unroll
        for (int i = 0; i < WIN - 5; ++i) kv[i] = __builtin_nontemporal_load(kc + kvbase + i * 64);
        __builtin_amdgcn_sched_barrier(0);
        __builtin_amdgcn_s_barrier();
        asm volatile("" ::: "memory");
#pragma unroll
        for (int i = WIN - 5; i < WIN; ++i) { flag_wait(lds, F_KGO + wv, i - (WIN - 5) + 1); kv[i] = __builtin_nontemporal_load(kc + kvbase + i * 64); __builtin_amdgcn_sched_barrier(0); }
        STAMP(1);
#pragma unroll
        for (int i = WIN; i < 16; ++i) { ada_wait(tstart, ADK_A + ADK_B * i); kv[i] = __builtin_nontemporal_load(kc + kvbase + i * 64); }
        __builtin_amdgcn_sched_barrier(0);
        flag_add(lds, F_KDONE, lane);
        release_credits<WIN>(lds, F_VGO + wv, lane);
        STAMP(2);
        flag_wait(lds, F_Q, 1);
        STAMP(3);
        const u32x4 q4 = *reinterpret_cast<const u32x4*>(lds + L_Q + c * 16);
        float xs[16];
#pragma unroll
        for (int i = 0; i < 16; ++i) {
            float d = 0.f;
#pragma unroll
            for (int k = 0; k < 4; ++k) d = dot2(kv[i][k], q4[k], d);
            xs[i] = d;
        }
#define TR_STEP(HALF, CTRL, BIT) _Pragma("unroll") for (int t_ = 0; t_ < (HALF); ++t_) { \
            const float lo_ = xs[t_], hi_ = xs[t_ + (HALF)]; const float t1_ = lo_ + dpp<CTRL>(lo_), t2_ = hi_ + dpp<CTRL>(hi_); \
            xs[t_] = (c & (BIT)) ? t2_ : t1_; }
        TR_STEP(8, ROR8, 8) TR_STEP(4, XOR7, 4) TR_STEP(2, XOR2, 2) TR_STEP(1, XOR1, 1)
#undef TR_STEP
        constexpr float C2 = 0.08838834764831845f * 1.4426950408889634f;
        const float sc = xs[0] * C2;
        float m = fmaxf(sc, dpp<XOR1>(sc));
        m = fmaxf(m, dpp<XOR2>(m));
        m = fmaxf(m, dpp<ROR4>(m)); m = fmaxf(m, dpp<ROR8>(m));
        m = swap16_max(m); m = swap32_max(m);
        const float p = __builtin_amdgcn_exp2f(sc - m);
        float l = p + dpp<XOR1>(p);
        l += dpp<XOR2>(l);
        l += dpp<ROR4>(l); l += dpp<ROR8>(l);
        l = swap16_add(l, l); l = swap32_add(l, l);
        const u32 ppk = pack_bf16(p, dpp<XOR1>(p));
        if ((c & 1) == 0) *reinterpret_cast<u32*>(lds + L_P + wv * 128 + r * 32 + (c >> 1) * 4) = ppk;
        if (lane == 0) { *reinterpret_cast<float*>(lds + L_M + wv * 4) = m; *reinterpret_cast<float*>(lds + L_L + wv * 4) = l; }
        flag_add(lds, F_P, lane);
        STAMP(4);
    } else if (wv < 8) {
        const int v = wv - 4;
        u32x4 vv[16];
        __builtin_amdgcn_s_barrier();
        asm volatile("" ::: "memory");
#pragma unroll
        for (int i = 0; i < WIN; ++i) { flag_wait(lds, F_VGO + v, i + 1); vv[i] = __builtin_nontemporal_load(vc + kvbase + i * 64); __builtin_amdgcn_sched_barrier(0); }
        STAMP(1);
#pragma unroll
        for (int i = WIN; i < 16; ++i) { ada_wait(tstart, ADA_A + ADA_B * i); vv[i] = __builtin_nontemporal_load(vc + kvbase + i * 64); }
        __builtin_amdgcn_sched_barrier(0);
        flag_add(lds, F_WGO + v, lane);
        STAMP(2);
#define VPERM(i_) _Pragma("unroll") for (int k = 0; k < 4; ++k) { \
            const u32 lo = __builtin_amdgcn_perm(vv[2 * (i_) + 1][k], vv[2 * (i_)][k], 0x05040100u); \
            const u32 hi = __builtin_amdgcn_perm(vv[2 * (i_) + 1][k], vv[2 * (i_)][k], 0x07060302u); \
            vv[2 * (i_)][k] = lo; vv[2 * (i_) + 1][k] = hi; }
#define VDOT(i_) { const u32 pp = (i_) < 4 ? pa[(i_) & 3] : pb[(i_) & 3]; _Pragma("unroll") for (int k = 0; k < 4; ++k) { \
            acc[2 * k] = dot2(vv[2 * (i_)][k], pp, acc[2 * k]); acc[2 * k + 1] = dot2(vv[2 * (i_) + 1][k], pp, acc[2 * k + 1]); } }
        VPERM(0) VPERM(1) VPERM(2) VPERM(3) VPERM(4)
#pragma unroll
        for (int i = 0; i < 10; ++i) asm volatile("" : "+v"(vv[i]));
        __builtin_amdgcn_sched_barrier(0);
        flag_wait(lds, F_P, 4);
        STAMP(3);
        const u32x4 pa = *reinterpret_cast<const u32x4*>(lds + L_P + v * 128 + r * 32);
        const u32x4 pb = *reinterpret_cast<const u32x4*>(lds + L_P + v * 128 + r * 32 + 16);
        const u32x2 tg = *reinterpret_cast<const u32x2*>(lds + L_TAG);
        const f32x4 m4 = *reinterpret_cast<const f32x4*>(lds + L_M);
        const float mown = *reinterpret_cast<const float*>(lds + L_M + v * 4);
        const float M = fmaxf(fmaxf(m4[0], m4[1]), fmaxf(m4[2], m4[3]));
        const float fv = __builtin_amdgcn_exp2f(mown - M);
        float acc[8];
#pragma unroll
        for (int k = 0; k < 8; ++k) acc[k] = 0.f;
        VDOT(0) VDOT(1) VDOT(2) VDOT(3) VDOT(4)
        __builtin_amdgcn_sched_barrier(0);
        VPERM(5) VDOT(5)
        __builtin_amdgcn_sched_barrier(0);
        VPERM(6) VDOT(6)
        __builtin_amdgcn_sched_barrier(0);
        VPERM(7) VDOT(7)
#undef VPERM
#undef VDOT
        {
            float u[4];
#pragma unroll
            for (int k = 0; k < 4; ++k) u[k] = swap32_add(acc[k], acc[k + 4]);
            f32x2 t;
            t[0] = swap16_add(u[0], u[2]) * fv; t[1] = swap16_add(u[1], u[3]) * fv;
            *reinterpret_cast<f32x2*>(lds + L_OP + v * 512 + (8 * c + 2 * r) * 4) = t;
        }
        STAMP(4);
        if (flag_add_ret(lds, F_OP, lane) == 3) {
            const int l5 = lane & 31, hf = lane >> 5;
            const f32x4 a0 = *reinterpret_cast<const f32x4*>(lds + L_OP + (2 * hf) * 512 + l5 * 16);
            const f32x4 a1 = *reinterpret_cast<const f32x4*>(lds + L_OP + (2 * hf + 1) * 512 + l5 * 16);
            const f32x4 a = a0 + a1;
            float o[4];
#pragma unroll
            for (int k = 0; k < 4; ++k) o[k] = swap32_add(a[k], a[k]);
            if (lane < 32) __builtin_amdgcn_raw_buffer_store_b128(enc4(o[0], o[1], o[2], o[3], tg[0], tg[1]), wsr,
                                                                  WS_X1 + (u32)(h * 8 + j) * X1_STRIDE + lane * 16, 0, 16);
            STAMP(5);
        }
    } else if (wv < NSW) {
        const int w4 = wv - 8;
        u32x4 wqv[5];
        const int xl = lane < 40 ? lane : 0;
        const u32x2 xv = *reinterpret_cast<const u32x2*>(x + (xl >= 20 ? D + (xl - 20) * 4 : xl * 4) + j * 80);
        __builtin_amdgcn_sched_barrier(0);
        {
            const u32x4* p = wq + (size_t)(j * 80 + w4 * 4 + r) * (H * DH / 8) + h * (DH / 8) + c;
#pragma unroll
            for (int i = 0; i < 5; ++i) wqv[i] = __builtin_nontemporal_load(p + (size_t)i * 16 * (H * DH / 8));
        }
        __builtin_amdgcn_sched_barrier(0);
        __builtin_amdgcn_s_barrier();
        asm volatile("" ::: "memory");
        release_credits<5>(lds, F_KGO + w4, lane);
        {
            float a0[8], a1[8];
#pragma unroll
            for (int k = 0; k < 8; ++k) { a0[k] = 0.f; a1[k] = 0.f; }
#pragma unroll
            for (int i = 0; i < 5; ++i) {
                const u32 xa0 = __builtin_amdgcn_readlane(xv[0], i * 4 + w4), xa1 = __builtin_amdgcn_readlane(xv[1], i * 4 + w4);
                const u32 xb0 = __builtin_amdgcn_readlane(xv[0], 20 + i * 4 + w4), xb1 = __builtin_amdgcn_readlane(xv[1], 20 + i * 4 + w4);
                const u32 sa = (r & 2) ? xa1 : xa0, sb = (r & 2) ? xb1 : xb0;
                const float x0 = (r & 1) ? bfhi(sa) : bflo(sa), x1 = (r & 1) ? bfhi(sb) : bflo(sb);
#pragma unroll
                for (int k = 0; k < 4; ++k) {
                    const float lo = bflo(wqv[i][k]), hi = bfhi(wqv[i][k]);
                    a0[2 * k] = fmaf(x0, lo, a0[2 * k]); a0[2 * k + 1] = fmaf(x0, hi, a0[2 * k + 1]);
                    a1[2 * k] = fmaf(x1, lo, a1[2 * k]); a1[2 * k + 1] = fmaf(x1, hi, a1[2 * k + 1]);
                }
            }
            float u[8];
#pragma unroll
            for (int k = 0; k < 8; ++k) u[k] = swap32_add(a0[k], a1[k]);
            f32x4 t;
#pragma unroll
            for (int k = 0; k < 4; ++k) t[k] = swap16_add(u[k], u[k + 4]);
            *reinterpret_cast<f32x4*>(lds + L_QP + w4 * 1024 + ((r >> 1) * 128 + 8 * c + 4 * (r & 1)) * 4) = t;
        }
        flag_add(lds, F_QP, lane);
        STAMP(1);
        const int t2 = tid - 512;
        const u32x4* wop[5];
#pragma unroll
        for (int i = 0; i < 5; ++i) {
            const u32 id = i * 256 + t2, row = id / 10, cc = id - row * 10;
            wop[i] = wo + (size_t)(h * DH + row) * (D / 8) + j * 10 + cc;
        }
        flag_wait(lds, F_WGO + w4, 1);
        u32x4 wov[5];
#pragma unroll
        for (int i = 0; i < 5; ++i) wov[i] = __builtin_nontemporal_load(wop[i]);
        __builtin_amdgcn_sched_barrier(0);
        STAMP(2);
#pragma unroll
        for (int i = 0; i < 5; ++i) *reinterpret_cast<u32x4*>(lds + L_WO + (i * 256 + t2) * 16) = wov[i];
        flag_add(lds, F_WO, lane);
        STAMP(3);
    } else {
        const u64 E = __hip_atomic_load(reinterpret_cast<u64*>(ws + WS_E), __ATOMIC_RELAXED, __HIP_MEMORY_SCOPE_AGENT);
        if (lane < 32) *reinterpret_cast<u32*>(lds + L_FLAG + lane * 4) = 0u;
        *reinterpret_cast<u32*>(lds + L_Z + lane * 4) = 0u;
        asm volatile("s_waitcnt lgkmcnt(0)" ::: "memory");
        __builtin_amdgcn_s_barrier();
        asm volatile("" ::: "memory");
        const u64 T = E + 1;
        const u32 T32 = __builtin_amdgcn_readfirstlane((u32)T);
        const u32 tagA = T32 & 0xffffu, tagB = ((T32 >> 16) + T32 * 40503u) & 0xffffu;
        if (lane == 0) *reinterpret_cast<u32x2*>(lds + L_TAG) = u32x2{tagA, tagB};
        const int l5 = lane & 31, hf = lane >> 5;
        flag_wait(lds, F_QP, 4);
        STAMP(1);
        {
            f32x4 a = *reinterpret_cast<const f32x4*>(lds + L_QP + lane * 16);
#pragma unroll
            for (int w = 1; w < 4; ++w) a += *reinterpret_cast<const f32x4*>(lds + L_QP + w * 1024 + lane * 16);
            __builtin_amdgcn_raw_buffer_store_b128(enc4(a[0], a[1], a[2], a[3], tagA, tagB), wsr, WS_XQ + (u32)(h * 8 + j) * 1024 + lane * 16, 0, 16);
        }
        u32 spins = 0;
        flag_wait(lds, F_KDONE, 4);
        {
            const u32 off = WS_XQ + (u32)(h * 8 + hf) * 1024 + b * 512 + l5 * 16;
            u32x4 gq[4];
            for (;;) {
                asm volatile("" ::: "memory");
                u32 bad = 0;
#pragma unroll
                for (int t = 0; t < 4; ++t) gq[t] = __builtin_amdgcn_raw_buffer_load_b128(wsr, off + t * 2048, 0, 16);
#pragma unroll
                for (int t = 0; t < 4; ++t) bad |= badg(gq[t], tagA, tagB);
                if (!__any(bad != 0) || ++spins > SPIN_CAP) break;
            }
            const f32x4 qs = (dec4(gq[0]) + dec4(gq[1])) + (dec4(gq[2]) + dec4(gq[3]));
            const float q0 = swap32_add(qs[0], qs[2]), q1 = swap32_add(qs[1], qs[3]);
            *reinterpret_cast<u32*>(lds + L_Q + l5 * 8 + hf * 4) = pack_bf16(q0, q1);
            flag_add(lds, F_Q, lane);
        }
        STAMP(2);
        if (X1WARM) {
            const u32x4 wrm = __builtin_amdgcn_raw_buffer_load_b128(wsr, WS_X1 + (u32)(h * 8 + j) * X1_STRIDE + l5 * 16, 0, 16);
            asm volatile("" :: "v"(wrm));
        }
        flag_wait(lds, F_P, 4);
        {
            const f32x4 m4 = *reinterpret_cast<const f32x4*>(lds + L_M);
            const f32x4 l4 = *reinterpret_cast<const f32x4*>(lds + L_L);
            const float M = fmaxf(fmaxf(m4[0], m4[1]), fmaxf(m4[2], m4[3]));
            const float Mhi = trunc24(M), Mlo = M - Mhi;
            float L = 0.f;
#pragma unroll
            for (int w = 0; w < 4; ++w) L = fmaf(__builtin_amdgcn_exp2f(m4[w] - M), l4[w], L);
            if (lane < 8) __builtin_amdgcn_raw_buffer_store_b128(enc4(Mhi, L, Mlo, L, tagA, tagB), wsr,
                                                                 WS_X1 + (u32)(h * 8 + j) * X1_STRIDE + 512 + lane * 16, 0, 16);
        }
        STAMP(3);
        flag_wait_fast(lds, F_OP, 4);
        STAMP(4);
        {
            const u32 off = WS_X1 + (u32)(h * 8) * X1_STRIDE;
            u32x4 go[4], gm;
            for (;;) {
                asm volatile("" ::: "memory");
                gm = __builtin_amdgcn_raw_buffer_load_b128(wsr, off + (lane & 7) * X1_STRIDE + 512, 0, 16);
#pragma unroll
                for (int t = 0; t < 4; ++t) go[t] = __builtin_amdgcn_raw_buffer_load_b128(wsr, off + (2 * t + hf) * X1_STRIDE + l5 * 16, 0, 16);
                u32 bad = badg(gm, tagA, tagB);
#pragma unroll
                for (int t = 0; t < 4; ++t) bad |= badg(go[t], tagA, tagB);
                if (!__any(bad != 0) || ++spins > SPIN_CAP) break;
            }
            const f32x4 ml = dec4(gm);
            const float ms = ml[0] + ml[2];
            float M = fmaxf(ms, dpp<XOR1>(ms)); M = fmaxf(M, dpp<XOR2>(M));
            const float f = __builtin_amdgcn_exp2f(ms - M);
            float L = f * ml[1]; L += dpp<XOR1>(L); L += dpp<XOR2>(L);
            const float fw = f * __builtin_amdgcn_rcpf(L);
            float wt[4];
#pragma unroll
            for (int t = 0; t < 4; ++t) {
                const float w0 = bitsf(__builtin_amdgcn_readlane(fbits(fw), 2 * t)), w1 = bitsf(__builtin_amdgcn_readlane(fbits(fw), 2 * t + 1));
                wt[t] = hf ? w1 : w0;
            }
            const f32x4 ob0 = dec4(go[0]) * wt[0] + dec4(go[1]) * wt[1];
            const f32x4 ob1 = dec4(go[2]) * wt[2] + dec4(go[3]) * wt[3];
            float o[4];
#pragma unroll
            for (int k = 0; k < 4; ++k) o[k] = swap32_add(ob0[k], ob1[k]);
            *reinterpret_cast<u32x2*>(lds + L_O + lane * 8) = u32x2{pack_bf16(o[0], o[1]), pack_bf16(o[2], o[3])};
            flag_add(lds, F_O, lane);
        }
        STAMP(5);
        const int rho = h / 6;
        if (h == rho * 6 && rho < 5) {
            const u32 off = WS_X2 + (u32)((j * 5 + rho) * 32) * 128 + lane * 16;
            u32x4 g0, g1, g2, g3;
            for (;;) {
                asm volatile("" ::: "memory");
                g0 = __builtin_amdgcn_raw_buffer_load_b128(wsr, off, 0, 16);
                g1 = __builtin_amdgcn_raw_buffer_load_b128(wsr, off + 1024, 0, 16);
                g2 = __builtin_amdgcn_raw_buffer_load_b128(wsr, off + 2048, 0, 16);
                g3 = __builtin_amdgcn_raw_buffer_load_b128(wsr, off + 3072, 0, 16);
                const u32 bad = badg(g0, tagA, tagB) | badg(g1, tagA, tagB) | badg(g2, tagA, tagB) | badg(g3, tagA, tagB);
                if (!__any(bad != 0)) break;
                if (++spins > SPIN_CAP) break;
            }
            STAMP(6);
            const f32x4 y4 = (dec4(g0) + dec4(g1)) + (dec4(g2) + dec4(g3));
            const float ya = swap32_add(y4[0], y4[1]), yb = swap32_add(y4[2], y4[3]);
            float y = swap16_add(ya, yb);
            y += dpp<ROR8>(y);
            if ((lane & 8) == 0) {
                const __bf16 ybf = (__bf16)y;
                out[hf * D + j * 80 + 16 * rho + 2 * (lane & 7) + ((lane >> 4) & 1)] = __builtin_bit_cast(unsigned short, ybf);
            }
            if (blockIdx.x == 0 && lane == 0) __hip_atomic_store(reinterpret_cast<u64*>(ws + WS_E), T, __ATOMIC_RELAXED, __HIP_MEMORY_SCOPE_AGENT);
        }
        STAMP(7);
    }
    if (wv < 4 || wv == 8) {
        const int nt = wv < 4 ? wv : 4;
        flag_wait(lds, F_WO, 4);
        const int q_ = (lane & 15) >> 2, p_ = lane & 3;
        const u32 tb = L_WO + (u32)(8 * r + q_) * 160 + 32 * nt + 8 * p_;
        s16x8 bf[4];
#pragma unroll
        for (int t = 0; t < 4; ++t) {
            const s16x4 b0 = __builtin_amdgcn_ds_read_tr16_b64_v4i16((LAS s16x4*)(lds + tb + t * 32 * 160));
            const s16x4 b1 = __builtin_amdgcn_ds_read_tr16_b64_v4i16((LAS s16x4*)(lds + tb + t * 32 * 160 + 4 * 160));
            bf[t] = s16x8{b0[0], b0[1], b0[2], b0[3], b1[0], b1[1], b1[2], b1[3]};
        }
        const u32x2 tg = *reinterpret_cast<const u32x2*>(lds + L_TAG);
        asm volatile("" : "+v"(bf[0]), "+v"(bf[1]), "+v"(bf[2]), "+v"(bf[3]));
        const u32 ab = (c < 2 ? L_O + c * 256 : L_Z) + 16 * r;
        flag_wait_fast(lds, F_O, 1);
        STAMP(6);
        f32x4 dacc = {0.f, 0.f, 0.f, 0.f}, dacc2 = {0.f, 0.f, 0.f, 0.f};
        u32x4 af[4];
#pragma unroll
        for (int t = 0; t < 4; ++t) af[t] = *reinterpret_cast<const u32x4*>(lds + ab + 64 * t);
        dacc = __builtin_amdgcn_mfma_f32_16x16x32_bf16(__builtin_bit_cast(bf16x8, af[0]), __builtin_bit_cast(bf16x8, bf[0]), dacc, 0, 0, 0);
        dacc2 = __builtin_amdgcn_mfma_f32_16x16x32_bf16(__builtin_bit_cast(bf16x8, af[1]), __builtin_bit_cast(bf16x8, bf[1]), dacc2, 0, 0, 0);
        dacc = __builtin_amdgcn_mfma_f32_16x16x32_bf16(__builtin_bit_cast(bf16x8, af[2]), __builtin_bit_cast(bf16x8, bf[2]), dacc, 0, 0, 0);
        dacc2 = __builtin_amdgcn_mfma_f32_16x16x32_bf16(__builtin_bit_cast(bf16x8, af[3]), __builtin_bit_cast(bf16x8, bf[3]), dacc2, 0, 0, 0);
        dacc += dacc2;
        const float n0 = dpp<XOR1>(dacc[0]), n1 = dpp<XOR1>(dacc[1]);
        if (lane < 16 && (lane & 1) == 0) {
            __builtin_amdgcn_raw_buffer_store_b128(enc4(dacc[0], dacc[1], n0, n1, tg[0], tg[1]), wsr,
                                                   WS_X2 + (u32)((j * 5 + nt) * 32 + h) * 128 + (lane >> 1) * 16, 0, 16);
        }
        STAMP(7);
    }
    if (blockIdx.x == 0 && wv == PROBE_W) {
        const u64 dl = (st[PROBE_B] - st[PROBE_A]) * 8;
        const u64 lim = dl < 20000 ? dl : 20000;
        while (__builtin_amdgcn_s_memrealtime() - st[0] < 2000 + lim) __builtin_amdgcn_s_sleep(2);
    }
}

extern "C" void kernel_launch(void* const* d_in, const int* in_sizes, int n_in,
                              void* d_out, int out_size, void* d_ws, size_t ws_size,
                              hipStream_t stream) {
    fused_decode<<<256, NTHR, 0, stream>>>((const unsigned short*)d_in[0], (const u32x4*)d_in[1], (const u32x4*)d_in[2],
                                           (const u32x4*)d_in[3], (const u32x4*)d_in[4], (unsigned short*)d_out,
                                           (unsigned char*)d_ws);
}
```

```cpp
#define PROBE_W 12
#define PROBE_A 0
#define PROBE_B 7
#define TOUCH 0
#include <hip/hip_runtime.h>
#include <hip/hip_bf16.h>
#include <cstdint>

typedef unsigned u32;
typedef unsigned long long u64;
typedef u32 u32x4 __attribute__((ext_vector_type(4)));
typedef u32 u32x2 __attribute__((ext_vector_type(2)));
typedef float f32x4 __attribute__((ext_vector_type(4)));
typedef float f32x2 __attribute__((ext_vector_type(2)));
typedef short s16x4 __attribute__((ext_vector_type(4)));
typedef short s16x8 __attribute__((ext_vector_type(8)));
typedef __bf16 bf16x2 __attribute__((ext_vector_type(2)));
typedef __bf16 bf16x8 __attribute__((ext_vector_type(8)));
#define LAS __attribute__((address_space(3)))

#define WIN 6
#define ADA 1
#define ADA_A 310
#define ADA_B 15
#define ADK_A 95
#define ADK_B 14
#define ADA_T 20
#define ADA_HI 9
#define ADA_LO 4
#define MICRO 0
#define X1WARM 1

constexpr int D = 640, H = 32, SK = 4096, DH = 128, W = 1024;
constexpr int NSW = 12, NTHR = (NSW + 1) * 64;

constexpr u32 WS_E = 0;
constexpr u32 WS_XQ = 4096;
constexpr u32 WS_X1 = WS_XQ + 32 * 8 * 1024;
constexpr u32 X1_STRIDE = 640;
constexpr u32 WS_X2 = WS_X1 + 32 * 8 * X1_STRIDE;
constexpr u32 WS_END = WS_X2 + 8 * 5 * 32 * 128;
constexpr u32 WS_STAMP = (WS_END + 4095) & ~4095u;
constexpr u32 SPIN_CAP = 1u << 20;

constexpr int L_QP = 0;
constexpr int L_OP = 4096;
constexpr int L_M = 6144;
constexpr int L_L = 6160;
constexpr int L_Q = 6208;
constexpr int L_O = 6464;
constexpr int L_TAG = 6976;
constexpr int L_FLAG = 6992;
constexpr int L_P = 7120;
constexpr int L_WO = 7632;
constexpr int L_Z = L_WO + 20480;
constexpr int L_END = L_Z + 256;
constexpr int F_QP = 0, F_Q = 1, F_OP = 2, F_O = 3, F_WO = 4, F_VGO = 5  , F_P = 9, F_KDONE = 14, F_KGO = 16  , F_WGO = 20  ;

__device__ __forceinline__ float bflo(u32 u) { return __builtin_bit_cast(float, u << 16); }
__device__ __forceinline__ float bfhi(u32 u) { return __builtin_bit_cast(float, u & 0xffff0000u); }
__device__ __forceinline__ u32 fbits(float f) { return __builtin_bit_cast(u32, f); }
__device__ __forceinline__ float bitsf(u32 u) { return __builtin_bit_cast(float, u); }
__device__ __forceinline__ u32 pack_bf16(float lo, float hi) {
    bf16x2 v = {(__bf16)lo, (__bf16)hi};
    return __builtin_bit_cast(u32, v);
}
__device__ __forceinline__ float dot2(u32 a, u32 b, float acc) {
    return __builtin_amdgcn_fdot2_f32_bf16(__builtin_bit_cast(bf16x2, a), __builtin_bit_cast(bf16x2, b), acc, false);
}
template <int CTRL> __device__ __forceinline__ float dpp(float x) {
    return __builtin_bit_cast(float, __builtin_amdgcn_mov_dpp(__builtin_bit_cast(int, x), CTRL, 0xf, 0xf, true));
}
constexpr int XOR1 = 0xB1, XOR2 = 0x4E, XOR7 = 0x141, ROR8 = 0x128, ROR4 = 0x124;
__device__ __forceinline__ float swap32_add(float a, float b) {
    auto s = __builtin_amdgcn_permlane32_swap(fbits(a), fbits(b), false, false);
    return bitsf(s[0]) + bitsf(s[1]);
}
__device__ __forceinline__ float swap16_add(float a, float b) {
    auto s = __builtin_amdgcn_permlane16_swap(fbits(a), fbits(b), false, false);
    return bitsf(s[0]) + bitsf(s[1]);
}
__device__ __forceinline__ float swap32_max(float a) {
    auto s = __builtin_amdgcn_permlane32_swap(fbits(a), fbits(a), false, false);
    return fmaxf(bitsf(s[0]), bitsf(s[1]));
}
__device__ __forceinline__ float swap16_max(float a) {
    auto s = __builtin_amdgcn_permlane16_swap(fbits(a), fbits(a), false, false);
    return fmaxf(bitsf(s[0]), bitsf(s[1]));
}
__device__ __forceinline__ float trunc24(float v) { return bitsf(fbits(v) & 0xffffff00u); }
__device__ __forceinline__ u32x4 enc4(float a, float b, float c, float d, u32 tagA, u32 tagB) {
    const u32 fa = fbits(a) + 0x80u, fb = fbits(b) + 0x80u, fc = fbits(c) + 0x80u, fd = fbits(d) + 0x80u;
    return u32x4{__builtin_amdgcn_perm(fb, fa, 0x05030201u), __builtin_amdgcn_perm(tagA, fb, 0x05040302u),
                 __builtin_amdgcn_perm(fd, fc, 0x05030201u), __builtin_amdgcn_perm(tagB, fd, 0x05040302u)};
}
__device__ __forceinline__ f32x4 dec4(u32x4 g) {
    return f32x4{bitsf(g[0] << 8), bitsf(__builtin_amdgcn_perm(g[1], g[0], 0x0504030cu)),
                 bitsf(g[2] << 8), bitsf(__builtin_amdgcn_perm(g[3], g[2], 0x0504030cu))};
}
__device__ __forceinline__ u32 badg(u32x4 g, u32 tagA, u32 tagB) { return ((g[1] >> 16) ^ tagA) | ((g[3] >> 16) ^ tagB); }

__device__ __forceinline__ void flag_add(unsigned char* lds, int f, int lane) {
    if (MICRO) asm volatile("" ::: "memory"); else asm volatile("s_waitcnt lgkmcnt(0)" ::: "memory");
    if (lane == 0) __hip_atomic_fetch_add((LAS u32*)(lds + L_FLAG) + f, 1u, __ATOMIC_RELAXED, __HIP_MEMORY_SCOPE_WORKGROUP);
    asm volatile("" ::: "memory");
}
__device__ __forceinline__ u32 flag_add_ret(unsigned char* lds, int f, int lane) {
    if (MICRO) asm volatile("" ::: "memory"); else asm volatile("s_waitcnt lgkmcnt(0)" ::: "memory");
    u32 old = 0;
    if (lane == 0) old = __hip_atomic_fetch_add((LAS u32*)(lds + L_FLAG) + f, 1u, __ATOMIC_RELAXED, __HIP_MEMORY_SCOPE_WORKGROUP);
    old = __builtin_amdgcn_readfirstlane(old);
    asm volatile("" ::: "memory");
    return old;
}
__device__ __forceinline__ u32 flag_get(unsigned char* lds, int f) {
    asm volatile("" ::: "memory");
    const u32 v = __hip_atomic_load((LAS u32*)(lds + L_FLAG) + f, __ATOMIC_RELAXED, __HIP_MEMORY_SCOPE_WORKGROUP);
    return __builtin_amdgcn_readfirstlane(v);
}
__device__ __forceinline__ void flag_wait(unsigned char* lds, int f, u32 want) {
#pragma nounroll
    for (u32 it = 0; it < (1u << 24); ++it) {
        if (flag_get(lds, f) >= want) break;
        __builtin_amdgcn_s_sleep(1);
    }
    asm volatile("" ::: "memory");
}
__device__ __forceinline__ void flag_wait_fast(unsigned char* lds, int f, u32 want) {
#pragma nounroll
    for (u32 it = 0; it < (1u << 26); ++it) {
        if (flag_get(lds, f) >= want) break;
        if (!MICRO) __builtin_amdgcn_s_sleep(1);
    }
    asm volatile("" ::: "memory");
}
template <int N> __device__ __forceinline__ void wait_vm() {
    __builtin_amdgcn_sched_barrier(0);
    asm volatile("s_waitcnt vmcnt(%0)" :: "n"(N) : "memory");
    __builtin_amdgcn_sched_barrier(0);
}
__device__ __forceinline__ void ada_wait(u64 tstart, int target) {
    if (ADA) {
        const int late = (int)(__builtin_amdgcn_s_memrealtime() - tstart) - target;
        if (late > ADA_T) wait_vm<ADA_HI - 1>(); else if (late < -ADA_T) wait_vm<ADA_LO - 1>(); else wait_vm<WIN - 1>();
    } else wait_vm<WIN - 1>();
}
template <int N> __device__ __forceinline__ void release_credits(unsigned char* lds, int f, int lane) {
    if constexpr (N > 0) { wait_vm<N - 1>(); flag_add(lds, f, lane); release_credits<N - 1>(lds, f, lane); }
}

#define STAMP(i) do { if ((i) == PROBE_A || (i) == PROBE_B) st[i] = __builtin_amdgcn_s_memrealtime(); } while (0)

__global__ void __launch_bounds__(NTHR) fused_decode(const unsigned short* __restrict__ x, const u32x4* __restrict__ kc,
                                                     const u32x4* __restrict__ vc, const u32x4* __restrict__ wq,
                                                     const u32x4* __restrict__ wo, unsigned short* __restrict__ out,
                                                     unsigned char* __restrict__ ws) {
    __shared__ __attribute__((aligned(16))) unsigned char lds[L_END];
    const int tid = threadIdx.x, lane = tid & 63;
    const int wv = __builtin_amdgcn_readfirstlane(tid >> 6);
    const int g = blockIdx.x & 7, idx = blockIdx.x >> 3;
    const int h = g * 4 + (idx >> 3), j = idx & 7, b = j >> 2, s = j & 3;
    const __amdgpu_buffer_rsrc_t wsr = __builtin_amdgcn_make_buffer_rsrc(ws, 0, WS_END, 0x00020000);
    asm volatile("" :: "s"(x), "s"(kc), "s"(vc), "s"(wq), "s"(wo), "s"(out), "s"(ws));
    u64 st[8] = {0, 0, 0, 0, 0, 0, 0, 0};
    STAMP(0);
    const u64 tstart = __builtin_amdgcn_s_memrealtime();
    const int r = lane >> 4, c = lane & 15;
    const size_t kvbase = ((size_t)(b * H + h) * SK + (SK - W) + s * 256 + (wv & 3) * 64) * (DH / 8) + lane;

    if (wv < 4) {
        u32x4 kv[16];
#pragma unroll
        for (int i = 0; i < WIN - 5; ++i) kv[i] = __builtin_nontemporal_load(kc + kvbase + i * 64);
        __builtin_amdgcn_sched_barrier(0);
        __builtin_amdgcn_s_barrier();
        asm volatile("" ::: "memory");
#pragma unroll
        for (int i = WIN - 5; i < WIN; ++i) { flag_wait(lds, F_KGO + wv, i - (WIN - 5) + 1); kv[i] = __builtin_nontemporal_load(kc + kvbase + i * 64); __builtin_amdgcn_sched_barrier(0); }
        STAMP(1);
#pragma unroll
        for (int i = WIN; i < 16; ++i) { ada_wait(tstart, ADK_A + ADK_B * i); kv[i] = __builtin_nontemporal_load(kc + kvbase + i * 64); }
        __builtin_amdgcn_sched_barrier(0);
        flag_add(lds, F_KDONE, lane);
        release_credits<WIN>(lds, F_VGO + wv, lane);
        STAMP(2);
        flag_wait(lds, F_Q, 1);
        STAMP(3);
        const u32x4 q4 = *reinterpret_cast<const u32x4*>(lds + L_Q + c * 16);
        float xs[16];
#pragma unroll
        for (int i = 0; i < 16; ++i) {
            float d = 0.f;
#pragma unroll
            for (int k = 0; k < 4; ++k) d = dot2(kv[i][k], q4[k], d);
            xs[i] = d;
        }
#define TR_STEP(HALF, CTRL, BIT) _Pragma("unroll") for (int t_ = 0; t_ < (HALF); ++t_) { \
            const float lo_ = xs[t_], hi_ = xs[t_ + (HALF)]; const float t1_ = lo_ + dpp<CTRL>(lo_), t2_ = hi_ + dpp<CTRL>(hi_); \
            xs[t_] = (c & (BIT)) ? t2_ : t1_; }
        TR_STEP(8, ROR8, 8) TR_STEP(4, XOR7, 4) TR_STEP(2, XOR2, 2) TR_STEP(1, XOR1, 1)
#undef TR_STEP
        constexpr float C2 = 0.08838834764831845f * 1.4426950408889634f;
        const float sc = xs[0] * C2;
        float m = fmaxf(sc, dpp<XOR1>(sc));
        m = fmaxf(m, dpp<XOR2>(m));
        m = fmaxf(m, dpp<ROR4>(m)); m = fmaxf(m, dpp<ROR8>(m));
        m = swap16_max(m); m = swap32_max(m);
        const float p = __builtin_amdgcn_exp2f(sc - m);
        float l = p + dpp<XOR1>(p);
        l += dpp<XOR2>(l);
        l += dpp<ROR4>(l); l += dpp<ROR8>(l);
        l = swap16_add(l, l); l = swap32_add(l, l);
        const u32 ppk = pack_bf16(p, dpp<XOR1>(p));
        if ((c & 1) == 0) *reinterpret_cast<u32*>(lds + L_P + wv * 128 + r * 32 + (c >> 1) * 4) = ppk;
        if (lane == 0) { *reinterpret_cast<float*>(lds + L_M + wv * 4) = m; *reinterpret_cast<float*>(lds + L_L + wv * 4) = l; }
        flag_add(lds, F_P, lane);
        STAMP(4);
    } else if (wv < 8) {
        const int v = wv - 4;
        u32x4 vv[16];
        u32 tch = 0;
        if (TOUCH && v == 0) {
            const u32* p = lane == 0 ? (const u32*)(vc + kvbase) : lane == 1 ? (const u32*)(wo + (size_t)(h * DH) * (D / 8) + j * 10)
                         : lane == 2 ? (const u32*)(wo + (size_t)(h * DH + 127) * (D / 8) + j * 10) : (const u32*)out;
            if (lane < 4) tch = __builtin_nontemporal_load(p);
        }
        __builtin_amdgcn_s_barrier();
        asm volatile("" ::: "memory");
        asm volatile("" :: "v"(tch));
#pragma unroll
        for (int i = 0; i < WIN; ++i) { flag_wait(lds, F_VGO + v, i + 1); vv[i] = __builtin_nontemporal_load(vc + kvbase + i * 64); __builtin_amdgcn_sched_barrier(0); }
        STAMP(1);
#pragma unroll
        for (int i = WIN; i < 16; ++i) { ada_wait(tstart, ADA_A + ADA_B * i); vv[i] = __builtin_nontemporal_load(vc + kvbase + i * 64); }
        __builtin_amdgcn_sched_barrier(0);
        flag_add(lds, F_WGO + v, lane);
        STAMP(2);
#define VPERM(i_) _Pragma("unroll") for (int k = 0; k < 4; ++k) { \
            const u32 lo = __builtin_amdgcn_perm(vv[2 * (i_) + 1][k], vv[2 * (i_)][k], 0x05040100u); \
            const u32 hi = __builtin_amdgcn_perm(vv[2 * (i_) + 1][k], vv[2 * (i_)][k], 0x07060302u); \
            vv[2 * (i_)][k] = lo; vv[2 * (i_) + 1][k] = hi; }
#define VDOT(i_) { const u32 pp = (i_) < 4 ? pa[(i_) & 3] : pb[(i_) & 3]; _Pragma("unroll") for (int k = 0; k < 4; ++k) { \
            acc[2 * k] = dot2(vv[2 * (i_)][k], pp, acc[2 * k]); acc[2 * k + 1] = dot2(vv[2 * (i_) + 1][k], pp, acc[2 * k + 1]); } }
        VPERM(0) VPERM(1) VPERM(2) VPERM(3) VPERM(4)
#pragma unroll
        for (int i = 0; i < 10; ++i) asm volatile("" : "+v"(vv[i]));
        __builtin_amdgcn_sched_barrier(0);
        flag_wait(lds, F_P, 4);
        STAMP(3);
        const u32x4 pa = *reinterpret_cast<const u32x4*>(lds + L_P + v * 128 + r * 32);
        const u32x4 pb = *reinterpret_cast<const u32x4*>(lds + L_P + v * 128 + r * 32 + 16);
        const u32x2 tg = *reinterpret_cast<const u32x2*>(lds + L_TAG);
        const f32x4 m4 = *reinterpret_cast<const f32x4*>(lds + L_M);
        const float mown = *reinterpret_cast<const float*>(lds + L_M + v * 4);
        const float M = fmaxf(fmaxf(m4[0], m4[1]), fmaxf(m4[2], m4[3]));
        const float fv = __builtin_amdgcn_exp2f(mown - M);
        float acc[8];
#pragma unroll
        for (int k = 0; k < 8; ++k) acc[k] = 0.f;
        VDOT(0) VDOT(1) VDOT(2) VDOT(3) VDOT(4)
        __builtin_amdgcn_sched_barrier(0);
        VPERM(5) VDOT(5)
        __builtin_amdgcn_sched_barrier(0);
        VPERM(6) VDOT(6)
        __builtin_amdgcn_sched_barrier(0);
        VPERM(7) VDOT(7)
#undef VPERM
#undef VDOT
        {
            float u[4];
#pragma unroll
            for (int k = 0; k < 4; ++k) u[k] = swap32_add(acc[k], acc[k + 4]);
            f32x2 t;
            t[0] = swap16_add(u[0], u[2]) * fv; t[1] = swap16_add(u[1], u[3]) * fv;
            *reinterpret_cast<f32x2*>(lds + L_OP + v * 512 + (8 * c + 2 * r) * 4) = t;
        }
        STAMP(4);
        if (flag_add_ret(lds, F_OP, lane) == 3) {
            const int l5 = lane & 31, hf = lane >> 5;
            const f32x4 a0 = *reinterpret_cast<const f32x4*>(lds + L_OP + (2 * hf) * 512 + l5 * 16);
            const f32x4 a1 = *reinterpret_cast<const f32x4*>(lds + L_OP + (2 * hf + 1) * 512 + l5 * 16);
            const f32x4 a = a0 + a1;
            float o[4];
#pragma unroll
            for (int k = 0; k < 4; ++k) o[k] = swap32_add(a[k], a[k]);
            if (lane < 32) __builtin_amdgcn_raw_buffer_store_b128(enc4(o[0], o[1], o[2], o[3], tg[0], tg[1]), wsr,
                                                                  WS_X1 + (u32)(h * 8 + j) * X1_STRIDE + lane * 16, 0, 16);
            STAMP(5);
        }
    } else if (wv < NSW) {
        const int w4 = wv - 8;
        u32x4 wqv[5];
        const int xl = lane < 40 ? lane : 0;
        const u32x2 xv = *reinterpret_cast<const u32x2*>(x + (xl >= 20 ? D + (xl - 20) * 4 : xl * 4) + j * 80);
        __builtin_amdgcn_sched_barrier(0);
        {
            const u32x4* p = wq + (size_t)(j * 80 + w4 * 4 + r) * (H * DH / 8) + h * (DH / 8) + c;
#pragma unroll
            for (int i = 0; i < 5; ++i) wqv[i] = __builtin_nontemporal_load(p + (size_t)i * 16 * (H * DH / 8));
        }
        __builtin_amdgcn_sched_barrier(0);
        __builtin_amdgcn_s_barrier();
        asm volatile("" ::: "memory");
        release_credits<5>(lds, F_KGO + w4, lane);
        {
            float a0[8], a1[8];
#pragma unroll
            for (int k = 0; k < 8; ++k) { a0[k] = 0.f; a1[k] = 0.f; }
#pragma unroll
            for (int i = 0; i < 5; ++i) {
                const u32 xa0 = __builtin_amdgcn_readlane(xv[0], i * 4 + w4), xa1 = __builtin_amdgcn_readlane(xv[1], i * 4 + w4);
                const u32 xb0 = __builtin_amdgcn_readlane(xv[0], 20 + i * 4 + w4), xb1 = __builtin_amdgcn_readlane(xv[1], 20 + i * 4 + w4);
                const u32 sa = (r & 2) ? xa1 : xa0, sb = (r & 2) ? xb1 : xb0;
                const float x0 = (r & 1) ? bfhi(sa) : bflo(sa), x1 = (r & 1) ? bfhi(sb) : bflo(sb);
#pragma unroll
                for (int k = 0; k < 4; ++k) {
                    const float lo = bflo(wqv[i][k]), hi = bfhi(wqv[i][k]);
                    a0[2 * k] = fmaf(x0, lo, a0[2 * k]); a0[2 * k + 1] = fmaf(x0, hi, a0[2 * k + 1]);
                    a1[2 * k] = fmaf(x1, lo, a1[2 * k]); a1[2 * k + 1] = fmaf(x1, hi, a1[2 * k + 1]);
                }
            }
            float u[8];
#pragma unroll
            for (int k = 0; k < 8; ++k) u[k] = swap32_add(a0[k], a1[k]);
            f32x4 t;
#pragma unroll
            for (int k = 0; k < 4; ++k) t[k] = swap16_add(u[k], u[k + 4]);
            *reinterpret_cast<f32x4*>(lds + L_QP + w4 * 1024 + ((r >> 1) * 128 + 8 * c + 4 * (r & 1)) * 4) = t;
        }
        flag_add(lds, F_QP, lane);
        STAMP(1);
        const int t2 = tid - 512;
        const u32x4* wop[5];
#pragma unroll
        for (int i = 0; i < 5; ++i) {
            const u32 id = i * 256 + t2, row = id / 10, cc = id - row * 10;
            wop[i] = wo + (size_t)(h * DH + row) * (D / 8) + j * 10 + cc;
        }
        flag_wait(lds, F_WGO + w4, 1);
        u32x4 wov[5];
#pragma unroll
        for (int i = 0; i < 5; ++i) wov[i] = __builtin_nontemporal_load(wop[i]);
        __builtin_amdgcn_sched_barrier(0);
        STAMP(2);
#pragma unroll
        for (int i = 0; i < 5; ++i) *reinterpret_cast<u32x4*>(lds + L_WO + (i * 256 + t2) * 16) = wov[i];
        flag_add(lds, F_WO, lane);
        STAMP(3);
    } else {
        const u64 E = __hip_atomic_load(reinterpret_cast<u64*>(ws + WS_E), __ATOMIC_RELAXED, __HIP_MEMORY_SCOPE_AGENT);
        if (lane < 32) *reinterpret_cast<u32*>(lds + L_FLAG + lane * 4) = 0u;
        *reinterpret_cast<u32*>(lds + L_Z + lane * 4) = 0u;
        asm volatile("s_waitcnt lgkmcnt(0)" ::: "memory");
        __builtin_amdgcn_s_barrier();
        asm volatile("" ::: "memory");
        const u64 T = E + 1;
        const u32 T32 = __builtin_amdgcn_readfirstlane((u32)T);
        const u32 tagA = T32 & 0xffffu, tagB = ((T32 >> 16) + T32 * 40503u) & 0xffffu;
        if (lane == 0) *reinterpret_cast<u32x2*>(lds + L_TAG) = u32x2{tagA, tagB};
        const int l5 = lane & 31, hf = lane >> 5;
        flag_wait(lds, F_QP, 4);
        STAMP(1);
        {
            f32x4 a = *reinterpret_cast<const f32x4*>(lds + L_QP + lane * 16);
#pragma unroll
            for (int w = 1; w < 4; ++w) a += *reinterpret_cast<const f32x4*>(lds + L_QP + w * 1024 + lane * 16);
            __builtin_amdgcn_raw_buffer_store_b128(enc4(a[0], a[1], a[2], a[3], tagA, tagB), wsr, WS_XQ + (u32)(h * 8 + j) * 1024 + lane * 16, 0, 16);
        }
        u32 spins = 0;
        flag_wait(lds, F_KDONE, 4);
        {
            const u32 off = WS_XQ + (u32)(h * 8 + hf) * 1024 + b * 512 + l5 * 16;
            u32x4 gq[4];
            for (;;) {
                asm volatile("" ::: "memory");
                u32 bad = 0;
#pragma unroll
                for (int t = 0; t < 4; ++t) gq[t] = __builtin_amdgcn_raw_buffer_load_b128(wsr, off + t * 2048, 0, 16);
#pragma unroll
                for (int t = 0; t < 4; ++t) bad |= badg(gq[t], tagA, tagB);
                if (!__any(bad != 0) || ++spins > SPIN_CAP) break;
            }
            const f32x4 qs = (dec4(gq[0]) + dec4(gq[1])) + (dec4(gq[2]) + dec4(gq[3]));
            const float q0 = swap32_add(qs[0], qs[2]), q1 = swap32_add(qs[1], qs[3]);
            *reinterpret_cast<u32*>(lds + L_Q + l5 * 8 + hf * 4) = pack_bf16(q0, q1);
            flag_add(lds, F_Q, lane);
        }
        STAMP(2);
        if (X1WARM) {
            const u32x4 wrm = __builtin_amdgcn_raw_buffer_load_b128(wsr, WS_X1 + (u32)(h * 8 + j) * X1_STRIDE + l5 * 16, 0, 16);
            asm volatile("" :: "v"(wrm));
        }
        flag_wait(lds, F_P, 4);
        {
            const f32x4 m4 = *reinterpret_cast<const f32x4*>(lds + L_M);
            const f32x4 l4 = *reinterpret_cast<const f32x4*>(lds + L_L);
            const float M = fmaxf(fmaxf(m4[0], m4[1]), fmaxf(m4[2], m4[3]));
            const float Mhi = trunc24(M), Mlo = M - Mhi;
            float L = 0.f;
#pragma unroll
            for (int w = 0; w < 4; ++w) L = fmaf(__builtin_amdgcn_exp2f(m4[w] - M), l4[w], L);
            if (lane < 8) __builtin_amdgcn_raw_buffer_store_b128(enc4(Mhi, L, Mlo, L, tagA, tagB), wsr,
                                                                 WS_X1 + (u32)(h * 8 + j) * X1_STRIDE + 512 + lane * 16, 0, 16);
        }
        STAMP(3);
        flag_wait_fast(lds, F_OP, 4);
        STAMP(4);
        {
            const u32 off = WS_X1 + (u32)(h * 8) * X1_STRIDE;
            u32x4 go[4], gm;
            for (;;) {
                asm volatile("" ::: "memory");
                gm = __builtin_amdgcn_raw_buffer_load_b128(wsr, off + (lane & 7) * X1_STRIDE + 512, 0, 16);
#pragma unroll
                for (int t = 0; t < 4; ++t) go[t] = __builtin_amdgcn_raw_buffer_load_b128(wsr, off + (2 * t + hf) * X1_STRIDE + l5 * 16, 0, 16);
                u32 bad = badg(gm, tagA, tagB);
#pragma unroll
                for (int t = 0; t < 4; ++t) bad |= badg(go[t], tagA, tagB);
                if (!__any(bad != 0) || ++spins > SPIN_CAP) break;
            }
            const f32x4 ml = dec4(gm);
            const float ms = ml[0] + ml[2];
            float M = fmaxf(ms, dpp<XOR1>(ms)); M = fmaxf(M, dpp<XOR2>(M));
            const float f = __builtin_amdgcn_exp2f(ms - M);
            float L = f * ml[1]; L += dpp<XOR1>(L); L += dpp<XOR2>(L);
            const float fw = f * __builtin_amdgcn_rcpf(L);
            float wt[4];
#pragma unroll
            for (int t = 0; t < 4; ++t) {
                const float w0 = bitsf(__builtin_amdgcn_readlane(fbits(fw), 2 * t)), w1 = bitsf(__builtin_amdgcn_readlane(fbits(fw), 2 * t + 1));
                wt[t] = hf ? w1 : w0;
            }
            const f32x4 ob0 = dec4(go[0]) * wt[0] + dec4(go[1]) * wt[1];
            const f32x4 ob1 = dec4(go[2]) * wt[2] + dec4(go[3]) * wt[3];
            float o[4];
#pragma unroll
            for (int k = 0; k < 4; ++k) o[k] = swap32_add(ob0[k], ob1[k]);
            *reinterpret_cast<u32x2*>(lds + L_O + lane * 8) = u32x2{pack_bf16(o[0], o[1]), pack_bf16(o[2], o[3])};
            flag_add(lds, F_O, lane);
        }
        STAMP(5);
        const int rho = h / 6;
        if (h == rho * 6 && rho < 5) {
            const u32 off = WS_X2 + (u32)((j * 5 + rho) * 32) * 128 + lane * 16;
            u32x4 g0, g1, g2, g3;
            for (;;) {
                asm volatile("" ::: "memory");
                g0 = __builtin_amdgcn_raw_buffer_load_b128(wsr, off, 0, 16);
                g1 = __builtin_amdgcn_raw_buffer_load_b128(wsr, off + 1024, 0, 16);
                g2 = __builtin_amdgcn_raw_buffer_load_b128(wsr, off + 2048, 0, 16);
                g3 = __builtin_amdgcn_raw_buffer_load_b128(wsr, off + 3072, 0, 16);
                const u32 bad = badg(g0, tagA, tagB) | badg(g1, tagA, tagB) | badg(g2, tagA, tagB) | badg(g3, tagA, tagB);
                if (!__any(bad != 0)) break;
                if (++spins > SPIN_CAP) break;
            }
            STAMP(6);
            const f32x4 y4 = (dec4(g0) + dec4(g1)) + (dec4(g2) + dec4(g3));
            const float ya = swap32_add(y4[0], y4[1]), yb = swap32_add(y4[2], y4[3]);
            float y = swap16_add(ya, yb);
            y += dpp<ROR8>(y);
            if ((lane & 8) == 0) {
                const __bf16 ybf = (__bf16)y;
                out[hf * D + j * 80 + 16 * rho + 2 * (lane & 7) + ((lane >> 4) & 1)] = __builtin_bit_cast(unsigned short, ybf);
            }
            if (blockIdx.x == 0 && lane == 0) __hip_atomic_store(reinterpret_cast<u64*>(ws + WS_E), T, __ATOMIC_RELAXED, __HIP_MEMORY_SCOPE_AGENT);
        }
        STAMP(7);
    }
    if (wv < 4 || wv == 8) {
        const int nt = wv < 4 ? wv : 4;
        flag_wait(lds, F_WO, 4);
        const int q_ = (lane & 15) >> 2, p_ = lane & 3;
        const u32 tb = L_WO + (u32)(8 * r + q_) * 160 + 32 * nt + 8 * p_;
        s16x8 bf[4];
#pragma unroll
        for (int t = 0; t < 4; ++t) {
            const s16x4 b0 = __builtin_amdgcn_ds_read_tr16_b64_v4i16((LAS s16x4*)(lds + tb + t * 32 * 160));
            const s16x4 b1 = __builtin_amdgcn_ds_read_tr16_b64_v4i16((LAS s16x4*)(lds + tb + t * 32 * 160 + 4 * 160));
            bf[t] = s16x8{b0[0], b0[1], b0[2], b0[3], b1[0], b1[1], b1[2], b1[3]};
        }
        const u32x2 tg = *reinterpret_cast<const u32x2*>(lds + L_TAG);
        asm volatile("" : "+v"(bf[0]), "+v"(bf[1]), "+v"(bf[2]), "+v"(bf[3]));
        const u32 ab = (c < 2 ? L_O + c * 256 : L_Z) + 16 * r;
        flag_wait_fast(lds, F_O, 1);
        STAMP(6);
        f32x4 dacc = {0.f, 0.f, 0.f, 0.f}, dacc2 = {0.f, 0.f, 0.f, 0.f};
        u32x4 af[4];
#pragma unroll
        for (int t = 0; t < 4; ++t) af[t] = *reinterpret_cast<const u32x4*>(lds + ab + 64 * t);
        dacc = __builtin_amdgcn_mfma_f32_16x16x32_bf16(__builtin_bit_cast(bf16x8, af[0]), __builtin_bit_cast(bf16x8, bf[0]), dacc, 0, 0, 0);
        dacc2 = __builtin_amdgcn_mfma_f32_16x16x32_bf16(__builtin_bit_cast(bf16x8, af[1]), __builtin_bit_cast(bf16x8, bf[1]), dacc2, 0, 0, 0);
        dacc = __builtin_amdgcn_mfma_f32_16x16x32_bf16(__builtin_bit_cast(bf16x8, af[2]), __builtin_bit_cast(bf16x8, bf[2]), dacc, 0, 0, 0);
        dacc2 = __builtin_amdgcn_mfma_f32_16x16x32_bf16(__builtin_bit_cast(bf16x8, af[3]), __builtin_bit_cast(bf16x8, bf[3]), dacc2, 0, 0, 0);
        dacc += dacc2;
        const float n0 = dpp<XOR1>(dacc[0]), n1 = dpp<XOR1>(dacc[1]);
        if (lane < 16 && (lane & 1) == 0) {
            __builtin_amdgcn_raw_buffer_store_b128(enc4(dacc[0], dacc[1], n0, n1, tg[0], tg[1]), wsr,
                                                   WS_X2 + (u32)((j * 5 + nt) * 32 + h) * 128 + (lane >> 1) * 16, 0, 16);
        }
        STAMP(7);
    }
    if (blockIdx.x == 0 && wv == PROBE_W) {
        const u64 dl = (st[PROBE_B] - st[PROBE_A]) * 8;
        const u64 lim = dl < 20000 ? dl : 20000;
        while (__builtin_amdgcn_s_memrealtime() - st[0] < 2000 + lim) __builtin_amdgcn_s_sleep(2);
    }
}

extern "C" void kernel_launch(void* const* d_in, const int* in_sizes, int n_in,
                              void* d_out, int out_size, void* d_ws, size_t ws_size,
                              hipStream_t stream) {
    fused_decode<<<256, NTHR, 0, stream>>>((const unsigned short*)d_in[0], (const u32x4*)d_in[1], (const u32x4*)d_in[2],
                                           (const u32x4*)d_in[3], (const u32x4*)d_in[4], (unsigned short*)d_out,
                                           (unsigned char*)d_ws);
}
```

```cpp
#include <hip/hip_runtime.h>
#include <hip/hip_bf16.h>
#include <cstdint>

typedef unsigned u32;
typedef unsigned long long u64;
typedef u32 u32x4 __attribute__((ext_vector_type(4)));
typedef u32 u32x2 __attribute__((ext_vector_type(2)));
typedef float f32x4 __attribute__((ext_vector_type(4)));
typedef float f32x2 __attribute__((ext_vector_type(2)));
typedef short s16x4 __attribute__((ext_vector_type(4)));
typedef short s16x8 __attribute__((ext_vector_type(8)));
typedef __bf16 bf16x2 __attribute__((ext_vector_type(2)));
typedef __bf16 bf16x8 __attribute__((ext_vector_type(8)));
#define LAS __attribute__((address_space(3)))

constexpr int WIN = 6;
constexpr int ADA_HI = 9, ADA_LO = 4;
constexpr int ADA_T = 20;
constexpr int ADK_A = 95, ADK_B = 14;
constexpr int ADA_A = 310, ADA_B = 15;

constexpr int D = 640, H = 32, SK = 4096, DH = 128, W = 1024;
constexpr int NSW = 12, NTHR = (NSW + 1) * 64;

constexpr u32 WS_E = 0;
constexpr u32 WS_XQ = 4096;
constexpr u32 WS_X1 = WS_XQ + 32 * 8 * 1024;
constexpr u32 X1_STRIDE = 640;
constexpr u32 WS_X2 = WS_X1 + 32 * 8 * X1_STRIDE;
constexpr u32 WS_END = WS_X2 + 8 * 5 * 32 * 128;
constexpr u32 WS_STAMP = (WS_END + 4095) & ~4095u;
constexpr u32 SPIN_CAP = 1u << 20;

constexpr int L_QP = 0;
constexpr int L_OP = 4096;
constexpr int L_M = 6144;
constexpr int L_L = 6160;
constexpr int L_Q = 6208;
constexpr int L_O = 6464;
constexpr int L_TAG = 6976;
constexpr int L_FLAG = 6992;
constexpr int L_P = 7120;
constexpr int L_WO = 7632;
constexpr int L_Z = L_WO + 20480;
constexpr int L_END = L_Z + 256;
constexpr int F_QP = 0, F_Q = 1, F_OP = 2, F_O = 3, F_WO = 4, F_VGO = 5  , F_P = 9, F_KDONE = 14, F_KGO = 16  , F_WGO = 20  ;

__device__ __forceinline__ float bflo(u32 u) { return __builtin_bit_cast(float, u << 16); }
__device__ __forceinline__ float bfhi(u32 u) { return __builtin_bit_cast(float, u & 0xffff0000u); }
__device__ __forceinline__ u32 fbits(float f) { return __builtin_bit_cast(u32, f); }
__device__ __forceinline__ float bitsf(u32 u) { return __builtin_bit_cast(float, u); }
__device__ __forceinline__ u32 pack_bf16(float lo, float hi) {
    bf16x2 v = {(__bf16)lo, (__bf16)hi};
    return __builtin_bit_cast(u32, v);
}
__device__ __forceinline__ float dot2(u32 a, u32 b, float acc) {
    return __builtin_amdgcn_fdot2_f32_bf16(__builtin_bit_cast(bf16x2, a), __builtin_bit_cast(bf16x2, b), acc, false);
}
template <int CTRL> __device__ __forceinline__ float dpp(float x) {
    return __builtin_bit_cast(float, __builtin_amdgcn_mov_dpp(__builtin_bit_cast(int, x), CTRL, 0xf, 0xf, true));
}
constexpr int XOR1 = 0xB1, XOR2 = 0x4E, XOR7 = 0x141, ROR8 = 0x128, ROR4 = 0x124;
__device__ __forceinline__ float swap32_add(float a, float b) {
    auto s = __builtin_amdgcn_permlane32_swap(fbits(a), fbits(b), false, false);
    return bitsf(s[0]) + bitsf(s[1]);
}
__device__ __forceinline__ float swap16_add(float a, float b) {
    auto s = __builtin_amdgcn_permlane16_swap(fbits(a), fbits(b), false, false);
    return bitsf(s[0]) + bitsf(s[1]);
}
__device__ __forceinline__ float swap32_max(float a) {
    auto s = __builtin_amdgcn_permlane32_swap(fbits(a), fbits(a), false, false);
    return fmaxf(bitsf(s[0]), bitsf(s[1]));
}
__device__ __forceinline__ float swap16_max(float a) {
    auto s = __builtin_amdgcn_permlane16_swap(fbits(a), fbits(a), false, false);
    return fmaxf(bitsf(s[0]), bitsf(s[1]));
}
__device__ __forceinline__ float trunc24(float v) { return bitsf(fbits(v) & 0xffffff00u); }
__device__ __forceinline__ u32x4 enc4(float a, float b, float c, float d, u32 tagA, u32 tagB) {
    const u32 fa = fbits(a) + 0x80u, fb = fbits(b) + 0x80u, fc = fbits(c) + 0x80u, fd = fbits(d) + 0x80u;
    return u32x4{__builtin_amdgcn_perm(fb, fa, 0x05030201u), __builtin_amdgcn_perm(tagA, fb, 0x05040302u),
                 __builtin_amdgcn_perm(fd, fc, 0x05030201u), __builtin_amdgcn_perm(tagB, fd, 0x05040302u)};
}
__device__ __forceinline__ f32x4 dec4(u32x4 g) {
    return f32x4{bitsf(g[0] << 8), bitsf(__builtin_amdgcn_perm(g[1], g[0], 0x0504030cu)),
                 bitsf(g[2] << 8), bitsf(__builtin_amdgcn_perm(g[3], g[2], 0x0504030cu))};
}
__device__ __forceinline__ u32 badg(u32x4 g, u32 tagA, u32 tagB) { return ((g[1] >> 16) ^ tagA) | ((g[3] >> 16) ^ tagB); }

__device__ __forceinline__ void flag_add(unsigned char* lds, int f, int lane) {
    asm volatile("s_waitcnt lgkmcnt(0)" ::: "memory");
    if (lane == 0) __hip_atomic_fetch_add((LAS u32*)(lds + L_FLAG) + f, 1u, __ATOMIC_RELAXED, __HIP_MEMORY_SCOPE_WORKGROUP);
    asm volatile("" ::: "memory");
}
__device__ __forceinline__ u32 flag_add_ret(unsigned char* lds, int f, int lane) {
    asm volatile("s_waitcnt lgkmcnt(0)" ::: "memory");
    u32 old = 0;
    if (lane == 0) old = __hip_atomic_fetch_add((LAS u32*)(lds + L_FLAG) + f, 1u, __ATOMIC_RELAXED, __HIP_MEMORY_SCOPE_WORKGROUP);
    old = __builtin_amdgcn_readfirstlane(old);
    asm volatile("" ::: "memory");
    return old;
}
__device__ __forceinline__ u32 flag_get(unsigned char* lds, int f) {
    asm volatile("" ::: "memory");
    const u32 v = __hip_atomic_load((LAS u32*)(lds + L_FLAG) + f, __ATOMIC_RELAXED, __HIP_MEMORY_SCOPE_WORKGROUP);
    return __builtin_amdgcn_readfirstlane(v);
}
__device__ __forceinline__ void flag_wait(unsigned char* lds, int f, u32 want) {
#pragma nounroll
    for (u32 it = 0; it < (1u << 24); ++it) {
        if (flag_get(lds, f) >= want) break;
        __builtin_amdgcn_s_sleep(1);
    }
    asm volatile("" ::: "memory");
}
template <int N> __device__ __forceinline__ void wait_vm() {
    __builtin_amdgcn_sched_barrier(0);
    asm volatile("s_waitcnt vmcnt(%0)" :: "n"(N) : "memory");
    __builtin_amdgcn_sched_barrier(0);
}
__device__ __forceinline__ void ada_wait(u64 tstart, int target) {
    const int late = (int)(__builtin_amdgcn_s_memrealtime() - tstart) - target;
    if (late > ADA_T) wait_vm<ADA_HI - 1>(); else if (late < -ADA_T) wait_vm<ADA_LO - 1>(); else wait_vm<WIN - 1>();
}
template <int N> __device__ __forceinline__ void release_credits(unsigned char* lds, int f, int lane) {
    if constexpr (N > 0) { wait_vm<N - 1>(); flag_add(lds, f, lane); release_credits<N - 1>(lds, f, lane); }
}

#define STAMP(i) do { } while (0)

__global__ void __launch_bounds__(NTHR) fused_decode(const unsigned short* __restrict__ x, const u32x4* __restrict__ kc,
                                                     const u32x4* __restrict__ vc, const u32x4* __restrict__ wq,
                                                     const u32x4* __restrict__ wo, unsigned short* __restrict__ out,
                                                     unsigned char* __restrict__ ws) {
    __shared__ __attribute__((aligned(16))) unsigned char lds[L_END];
    const int tid = threadIdx.x, lane = tid & 63;
    const int hw = __builtin_amdgcn_readfirstlane(tid >> 6);
    const int wv = hw == 0 ? 12 : hw <= 4 ? hw + 7 : hw - 5;
    const int g = blockIdx.x & 7, idx = blockIdx.x >> 3;
    const int h = g * 4 + (idx >> 3), j = idx & 7, b = j >> 2, s = j & 3;
    const __amdgpu_buffer_rsrc_t wsr = __builtin_amdgcn_make_buffer_rsrc(ws, 0, WS_END, 0x00020000);
    asm volatile("" :: "s"(x), "s"(kc), "s"(vc), "s"(wq), "s"(wo), "s"(out), "s"(ws));
    STAMP(0);
    const u64 tstart = __builtin_amdgcn_s_memrealtime();
    const int r = lane >> 4, c = lane & 15;
    const size_t kvbase = ((size_t)(b * H + h) * SK + (SK - W) + s * 256 + (wv & 3) * 64) * (DH / 8) + lane;

    if (wv < 4) {
        u32x4 kv[16];
#pragma unroll
        for (int i = 0; i < WIN - 5; ++i) kv[i] = __builtin_nontemporal_load(kc + kvbase + i * 64);
        __builtin_amdgcn_sched_barrier(0);
        __builtin_amdgcn_s_barrier();
        asm volatile("" ::: "memory");
#pragma unroll
        for (int i = WIN - 5; i < WIN; ++i) { flag_wait(lds, F_KGO + wv, i - (WIN - 5) + 1); kv[i] = __builtin_nontemporal_load(kc + kvbase + i * 64); __builtin_amdgcn_sched_barrier(0); }
        STAMP(1);
#pragma unroll
        for (int i = WIN; i < 16; ++i) { ada_wait(tstart, ADK_A + ADK_B * i); kv[i] = __builtin_nontemporal_load(kc + kvbase + i * 64); }
        __builtin_amdgcn_sched_barrier(0);
        flag_add(lds, F_KDONE, lane);
        release_credits<WIN>(lds, F_VGO + wv, lane);
        STAMP(2);
        flag_wait(lds, F_Q, 1);
        STAMP(3);
        const u32x4 q4 = *reinterpret_cast<const u32x4*>(lds + L_Q + c * 16);
        float xs[16];
#pragma unroll
        for (int i = 0; i < 16; ++i) {
            float d = 0.f;
#pragma unroll
            for (int k = 0; k < 4; ++k) d = dot2(kv[i][k], q4[k], d);
            xs[i] = d;
        }
#define TR_STEP(HALF, CTRL, BIT) _Pragma("unroll") for (int t_ = 0; t_ < (HALF); ++t_) { \
            const float lo_ = xs[t_], hi_ = xs[t_ + (HALF)]; const float t1_ = lo_ + dpp<CTRL>(lo_), t2_ = hi_ + dpp<CTRL>(hi_); \
            xs[t_] = (c & (BIT)) ? t2_ : t1_; }
        TR_STEP(8, ROR8, 8) TR_STEP(4, XOR7, 4) TR_STEP(2, XOR2, 2) TR_STEP(1, XOR1, 1)
#undef TR_STEP
        constexpr float C2 = 0.08838834764831845f * 1.4426950408889634f;
        const float sc = xs[0] * C2;
        float m = fmaxf(sc, dpp<XOR1>(sc));
        m = fmaxf(m, dpp<XOR2>(m));
        m = fmaxf(m, dpp<ROR4>(m)); m = fmaxf(m, dpp<ROR8>(m));
        m = swap16_max(m); m = swap32_max(m);
        const float p = __builtin_amdgcn_exp2f(sc - m);
        float l = p + dpp<XOR1>(p);
        l += dpp<XOR2>(l);
        l += dpp<ROR4>(l); l += dpp<ROR8>(l);
        l = swap16_add(l, l); l = swap32_add(l, l);
        const u32 ppk = pack_bf16(p, dpp<XOR1>(p));
        if ((c & 1) == 0) *reinterpret_cast<u32*>(lds + L_P + wv * 128 + r * 32 + (c >> 1) * 4) = ppk;
        if (lane == 0) { *reinterpret_cast<float*>(lds + L_M + wv * 4) = m; *reinterpret_cast<float*>(lds + L_L + wv * 4) = l; }
        flag_add(lds, F_P, lane);
        STAMP(4);
    } else if (wv < 8) {
        const int v = wv - 4;
        u32x4 vv[16];
        u32 tch = 0;
        if (v == 0) {
            const u32* p = lane == 0 ? (const u32*)(vc + kvbase) : lane == 1 ? (const u32*)(wo + (size_t)(h * DH) * (D / 8) + j * 10)
                         : lane == 2 ? (const u32*)(wo + (size_t)(h * DH + 127) * (D / 8) + j * 10) : (const u32*)out;
            if (lane < 4) tch = __builtin_nontemporal_load(p);
        }
        __builtin_amdgcn_s_barrier();
        asm volatile("" ::: "memory");
        asm volatile("" :: "v"(tch));
#pragma unroll
        for (int i = 0; i < WIN; ++i) { flag_wait(lds, F_VGO + v, i + 1); vv[i] = __builtin_nontemporal_load(vc + kvbase + i * 64); __builtin_amdgcn_sched_barrier(0); }
        STAMP(1);
#pragma unroll
        for (int i = WIN; i < 16; ++i) { ada_wait(tstart, ADA_A + ADA_B * i); vv[i] = __builtin_nontemporal_load(vc + kvbase + i * 64); }
        __builtin_amdgcn_sched_barrier(0);
        flag_add(lds, F_WGO + v, lane);
        STAMP(2);
#define VPERM(i_) _Pragma("unroll") for (int k = 0; k < 4; ++k) { \
            const u32 lo = __builtin_amdgcn_perm(vv[2 * (i_) + 1][k], vv[2 * (i_)][k], 0x05040100u); \
            const u32 hi = __builtin_amdgcn_perm(vv[2 * (i_) + 1][k], vv[2 * (i_)][k], 0x07060302u); \
            vv[2 * (i_)][k] = lo; vv[2 * (i_) + 1][k] = hi; }
#define VDOT(i_) { const u32 pp = (i_) < 4 ? pa[(i_) & 3] : pb[(i_) & 3]; _Pragma("unroll") for (int k = 0; k < 4; ++k) { \
            acc[2 * k] = dot2(vv[2 * (i_)][k], pp, acc[2 * k]); acc[2 * k + 1] = dot2(vv[2 * (i_) + 1][k], pp, acc[2 * k + 1]); } }
        VPERM(0) VPERM(1) VPERM(2) VPERM(3) VPERM(4)
#pragma unroll
        for (int i = 0; i < 10; ++i) asm volatile("" : "+v"(vv[i]));
        __builtin_amdgcn_sched_barrier(0);
        flag_wait(lds, F_P, 4);
        STAMP(3);
        const u32x4 pa = *reinterpret_cast<const u32x4*>(lds + L_P + v * 128 + r * 32);
        const u32x4 pb = *reinterpret_cast<const u32x4*>(lds + L_P + v * 128 + r * 32 + 16);
        const u32x2 tg = *reinterpret_cast<const u32x2*>(lds + L_TAG);
        const f32x4 m4 = *reinterpret_cast<const f32x4*>(lds + L_M);
        const float mown = *reinterpret_cast<const float*>(lds + L_M + v * 4);
        const float M = fmaxf(fmaxf(m4[0], m4[1]), fmaxf(m4[2], m4[3]));
        const float fv = __builtin_amdgcn_exp2f(mown - M);
        float acc[8];
#pragma unroll
        for (int k = 0; k < 8; ++k) acc[k] = 0.f;
        VDOT(0) VDOT(1) VDOT(2) VDOT(3) VDOT(4)
        __builtin_amdgcn_sched_barrier(0);
        VPERM(5) VDOT(5)
        __builtin_amdgcn_sched_barrier(0);
        VPERM(6) VDOT(6)
        __builtin_amdgcn_sched_barrier(0);
        VPERM(7) VDOT(7)
#undef VPERM
#undef VDOT
        {
            float u[4];
#pragma unroll
            for (int k = 0; k < 4; ++k) u[k] = swap32_add(acc[k], acc[k + 4]);
            f32x2 t;
            t[0] = swap16_add(u[0], u[2]) * fv; t[1] = swap16_add(u[1], u[3]) * fv;
            *reinterpret_cast<f32x2*>(lds + L_OP + v * 512 + (8 * c + 2 * r) * 4) = t;
        }
        STAMP(4);
        if (flag_add_ret(lds, F_OP, lane) == 3) {
            const int l5 = lane & 31, hf = lane >> 5;
            const f32x4 a0 = *reinterpret_cast<const f32x4*>(lds + L_OP + (2 * hf) * 512 + l5 * 16);
            const f32x4 a1 = *reinterpret_cast<const f32x4*>(lds + L_OP + (2 * hf + 1) * 512 + l5 * 16);
            const f32x4 a = a0 + a1;
            float o[4];
#pragma unroll
            for (int k = 0; k < 4; ++k) o[k] = swap32_add(a[k], a[k]);
            if (lane < 32) __builtin_amdgcn_raw_buffer_store_b128(enc4(o[0], o[1], o[2], o[3], tg[0], tg[1]), wsr,
                                                                  WS_X1 + (u32)(h * 8 + j) * X1_STRIDE + lane * 16, 0, 16);
            STAMP(5);
        }
    } else if (wv < NSW) {
        const int w4 = wv - 8;
        u32x4 wqv[5];
        const int xl = lane < 40 ? lane : 0;
        const u32x2 xv = *reinterpret_cast<const u32x2*>(x + (xl >= 20 ? D + (xl - 20) * 4 : xl * 4) + j * 80);
        __builtin_amdgcn_sched_barrier(0);
        {
            const u32x4* p = wq + (size_t)(j * 80 + w4 * 4 + r) * (H * DH / 8) + h * (DH / 8) + c;
#pragma unroll
            for (int i = 0; i < 5; ++i) wqv[i] = __builtin_nontemporal_load(p + (size_t)i * 16 * (H * DH / 8));
        }
        __builtin_amdgcn_sched_barrier(0);
        __builtin_amdgcn_s_barrier();
        asm volatile("" ::: "memory");
        release_credits<5>(lds, F_KGO + w4, lane);
        {
            float a0[8], a1[8];
#pragma unroll
            for (int k = 0; k < 8; ++k) { a0[k] = 0.f; a1[k] = 0.f; }
#pragma unroll
            for (int i = 0; i < 5; ++i) {
                const u32 xa0 = __builtin_amdgcn_readlane(xv[0], i * 4 + w4), xa1 = __builtin_amdgcn_readlane(xv[1], i * 4 + w4);
                const u32 xb0 = __builtin_amdgcn_readlane(xv[0], 20 + i * 4 + w4), xb1 = __builtin_amdgcn_readlane(xv[1], 20 + i * 4 + w4);
                const u32 sa = (r & 2) ? xa1 : xa0, sb = (r & 2) ? xb1 : xb0;
                const float x0 = (r & 1) ? bfhi(sa) : bflo(sa), x1 = (r & 1) ? bfhi(sb) : bflo(sb);
#pragma unroll
                for (int k = 0; k < 4; ++k) {
                    const float lo = bflo(wqv[i][k]), hi = bfhi(wqv[i][k]);
                    a0[2 * k] = fmaf(x0, lo, a0[2 * k]); a0[2 * k + 1] = fmaf(x0, hi, a0[2 * k + 1]);
                    a1[2 * k] = fmaf(x1, lo, a1[2 * k]); a1[2 * k + 1] = fmaf(x1, hi, a1[2 * k + 1]);
                }
            }
            float u[8];
#pragma unroll
            for (int k = 0; k < 8; ++k) u[k] = swap32_add(a0[k], a1[k]);
            f32x4 t;
#pragma unroll
            for (int k = 0; k < 4; ++k) t[k] = swap16_add(u[k], u[k + 4]);
            *reinterpret_cast<f32x4*>(lds + L_QP + w4 * 1024 + ((r >> 1) * 128 + 8 * c + 4 * (r & 1)) * 4) = t;
        }
        flag_add(lds, F_QP, lane);
        STAMP(1);
        const int t2 = w4 * 64 + lane;
        const u32x4* wop[5];
#pragma unroll
        for (int i = 0; i < 5; ++i) {
            const u32 id = i * 256 + t2, row = id / 10, cc = id - row * 10;
            wop[i] = wo + (size_t)(h * DH + row) * (D / 8) + j * 10 + cc;
        }
        flag_wait(lds, F_WGO + w4, 1);
        u32x4 wov[5];
#pragma unroll
        for (int i = 0; i < 5; ++i) wov[i] = __builtin_nontemporal_load(wop[i]);
        __builtin_amdgcn_sched_barrier(0);
        STAMP(2);
#pragma unroll
        for (int i = 0; i < 5; ++i) *reinterpret_cast<u32x4*>(lds + L_WO + (i * 256 + t2) * 16) = wov[i];
        flag_add(lds, F_WO, lane);
        STAMP(3);
    } else {
        __builtin_amdgcn_s_setprio(3);
        const u64 E = __hip_atomic_load(reinterpret_cast<u64*>(ws + WS_E), __ATOMIC_RELAXED, __HIP_MEMORY_SCOPE_AGENT);
        if (lane < 32) *reinterpret_cast<u32*>(lds + L_FLAG + lane * 4) = 0u;
        *reinterpret_cast<u32*>(lds + L_Z + lane * 4) = 0u;
        asm volatile("s_waitcnt lgkmcnt(0)" ::: "memory");
        __builtin_amdgcn_s_barrier();
        asm volatile("" ::: "memory");
        const u64 T = E + 1;
        const u32 T32 = __builtin_amdgcn_readfirstlane((u32)T);
        const u32 tagA = T32 & 0xffffu, tagB = ((T32 >> 16) + T32 * 40503u) & 0xffffu;
        if (lane == 0) *reinterpret_cast<u32x2*>(lds + L_TAG) = u32x2{tagA, tagB};
        const int l5 = lane & 31, hf = lane >> 5;
        flag_wait(lds, F_QP, 4);
        STAMP(1);
        {
            f32x4 a = *reinterpret_cast<const f32x4*>(lds + L_QP + lane * 16);
#pragma unroll
            for (int w = 1; w < 4; ++w) a += *reinterpret_cast<const f32x4*>(lds + L_QP + w * 1024 + lane * 16);
            __builtin_amdgcn_raw_buffer_store_b128(enc4(a[0], a[1], a[2], a[3], tagA, tagB), wsr, WS_XQ + (u32)(h * 8 + j) * 1024 + lane * 16, 0, 16);
        }
        u32 spins = 0;
        flag_wait(lds, F_KDONE, 4);
        {
            const u32 off = WS_XQ + (u32)(h * 8 + hf) * 1024 + b * 512 + l5 * 16;
            u32x4 gq[4];
            for (;;) {
                asm volatile("" ::: "memory");
                u32 bad = 0;
#pragma unroll
                for (int t = 0; t < 4; ++t) gq[t] = __builtin_amdgcn_raw_buffer_load_b128(wsr, off + t * 2048, 0, 16);
#pragma unroll
                for (int t = 0; t < 4; ++t) bad |= badg(gq[t], tagA, tagB);
                if (!__any(bad != 0) || ++spins > SPIN_CAP) break;
            }
            const f32x4 qs = (dec4(gq[0]) + dec4(gq[1])) + (dec4(gq[2]) + dec4(gq[3]));
            const float q0 = swap32_add(qs[0], qs[2]), q1 = swap32_add(qs[1], qs[3]);
            *reinterpret_cast<u32*>(lds + L_Q + l5 * 8 + hf * 4) = pack_bf16(q0, q1);
            flag_add(lds, F_Q, lane);
        }
        STAMP(2);
        {
            const u32x4 wrm = __builtin_amdgcn_raw_buffer_load_b128(wsr, WS_X1 + (u32)(h * 8 + j) * X1_STRIDE + l5 * 16, 0, 16);
            asm volatile("" :: "v"(wrm));
        }
        flag_wait(lds, F_P, 4);
        {
            const f32x4 m4 = *reinterpret_cast<const f32x4*>(lds + L_M);
            const f32x4 l4 = *reinterpret_cast<const f32x4*>(lds + L_L);
            const float M = fmaxf(fmaxf(m4[0], m4[1]), fmaxf(m4[2], m4[3]));
            const float Mhi = trunc24(M), Mlo = M - Mhi;
            float L = 0.f;
#pragma unroll
            for (int w = 0; w < 4; ++w) L = fmaf(__builtin_amdgcn_exp2f(m4[w] - M), l4[w], L);
            if (lane < 8) __builtin_amdgcn_raw_buffer_store_b128(enc4(Mhi, L, Mlo, L, tagA, tagB), wsr,
                                                                 WS_X1 + (u32)(h * 8 + j) * X1_STRIDE + 512 + lane * 16, 0, 16);
        }
        STAMP(3);
        flag_wait(lds, F_OP, 4);
        STAMP(4);
        {
            const u32 off = WS_X1 + (u32)(h * 8) * X1_STRIDE;
            u32x4 go[4], gm;
            for (;;) {
                asm volatile("" ::: "memory");
                gm = __builtin_amdgcn_raw_buffer_load_b128(wsr, off + (lane & 7) * X1_STRIDE + 512, 0, 16);
#pragma unroll
                for (int t = 0; t < 4; ++t) go[t] = __builtin_amdgcn_raw_buffer_load_b128(wsr, off + (2 * t + hf) * X1_STRIDE + l5 * 16, 0, 16);
                u32 bad = badg(gm, tagA, tagB);
#pragma unroll
                for (int t = 0; t < 4; ++t) bad |= badg(go[t], tagA, tagB);
                if (!__any(bad != 0) || ++spins > SPIN_CAP) break;
            }
            const f32x4 ml = dec4(gm);
            const float ms = ml[0] + ml[2];
            float M = fmaxf(ms, dpp<XOR1>(ms)); M = fmaxf(M, dpp<XOR2>(M));
            const float f = __builtin_amdgcn_exp2f(ms - M);
            float L = f * ml[1]; L += dpp<XOR1>(L); L += dpp<XOR2>(L);
            const float fw = f * __builtin_amdgcn_rcpf(L);
            float wt[4];
#pragma unroll
            for (int t = 0; t < 4; ++t) {
                const float w0 = bitsf(__builtin_amdgcn_readlane(fbits(fw), 2 * t)), w1 = bitsf(__builtin_amdgcn_readlane(fbits(fw), 2 * t + 1));
                wt[t] = hf ? w1 : w0;
            }
            const f32x4 ob0 = dec4(go[0]) * wt[0] + dec4(go[1]) * wt[1];
            const f32x4 ob1 = dec4(go[2]) * wt[2] + dec4(go[3]) * wt[3];
            float o[4];
#pragma unroll
            for (int k = 0; k < 4; ++k) o[k] = swap32_add(ob0[k], ob1[k]);
            *reinterpret_cast<u32x2*>(lds + L_O + lane * 8) = u32x2{pack_bf16(o[0], o[1]), pack_bf16(o[2], o[3])};
            flag_add(lds, F_O, lane);
        }
        STAMP(5);
        const int rho = h / 6;
        if (h == rho * 6 && rho < 5) {
            const u32 off = WS_X2 + (u32)((j * 5 + rho) * 32) * 128 + lane * 16;
            u32x4 g0, g1, g2, g3;
            for (;;) {
                asm volatile("" ::: "memory");
                g0 = __builtin_amdgcn_raw_buffer_load_b128(wsr, off, 0, 16);
                g1 = __builtin_amdgcn_raw_buffer_load_b128(wsr, off + 1024, 0, 16);
                g2 = __builtin_amdgcn_raw_buffer_load_b128(wsr, off + 2048, 0, 16);
                g3 = __builtin_amdgcn_raw_buffer_load_b128(wsr, off + 3072, 0, 16);
                const u32 bad = badg(g0, tagA, tagB) | badg(g1, tagA, tagB) | badg(g2, tagA, tagB) | badg(g3, tagA, tagB);
                if (!__any(bad != 0)) break;
                if (++spins > SPIN_CAP) break;
            }
            STAMP(6);
            const f32x4 y4 = (dec4(g0) + dec4(g1)) + (dec4(g2) + dec4(g3));
            const float ya = swap32_add(y4[0], y4[1]), yb = swap32_add(y4[2], y4[3]);
            float y = swap16_add(ya, yb);
            y += dpp<ROR8>(y);
            if ((lane & 8) == 0) {
                const __bf16 ybf = (__bf16)y;
                out[hf * D + j * 80 + 16 * rho + 2 * (lane & 7) + ((lane >> 4) & 1)] = __builtin_bit_cast(unsigned short, ybf);
            }
            if (blockIdx.x == 0 && lane == 0) __hip_atomic_store(reinterpret_cast<u64*>(ws + WS_E), T, __ATOMIC_RELAXED, __HIP_MEMORY_SCOPE_AGENT);
        }
        STAMP(7);
    }
    if (wv < 4 || wv == 8) {
        const int nt = wv < 4 ? wv : 4;
        flag_wait(lds, F_WO, 4);
        const int q_ = (lane & 15) >> 2, p_ = lane & 3;
        const u32 tb = L_WO + (u32)(8 * r + q_) * 160 + 32 * nt + 8 * p_;
        s16x8 bf[4];
#pragma unroll
        for (int t = 0; t < 4; ++t) {
            const s16x4 b0 = __builtin_amdgcn_ds_read_tr16_b64_v4i16((LAS s16x4*)(lds + tb + t * 32 * 160));
            const s16x4 b1 = __builtin_amdgcn_ds_read_tr16_b64_v4i16((LAS s16x4*)(lds + tb + t * 32 * 160 + 4 * 160));
            bf[t] = s16x8{b0[0], b0[1], b0[2], b0[3], b1[0], b1[1], b1[2], b1[3]};
        }
        const u32x2 tg = *reinterpret_cast<const u32x2*>(lds + L_TAG);
        asm volatile("" : "+v"(bf[0]), "+v"(bf[1]), "+v"(bf[2]), "+v"(bf[3]));
        const u32 ab = (c < 2 ? L_O + c * 256 : L_Z) + 16 * r;
        flag_wait(lds, F_O, 1);
        STAMP(6);
        f32x4 dacc = {0.f, 0.f, 0.f, 0.f}, dacc2 = {0.f, 0.f, 0.f, 0.f};
        u32x4 af[4];
#pragma unroll
        for (int t = 0; t < 4; ++t) af[t] = *reinterpret_cast<const u32x4*>(lds + ab + 64 * t);
        dacc = __builtin_amdgcn_mfma_f32_16x16x32_bf16(__builtin_bit_cast(bf16x8, af[0]), __builtin_bit_cast(bf16x8, bf[0]), dacc, 0, 0, 0);
        dacc2 = __builtin_amdgcn_mfma_f32_16x16x32_bf16(__builtin_bit_cast(bf16x8, af[1]), __builtin_bit_cast(bf16x8, bf[1]), dacc2, 0, 0, 0);
        dacc = __builtin_amdgcn_mfma_f32_16x16x32_bf16(__builtin_bit_cast(bf16x8, af[2]), __builtin_bit_cast(bf16x8, bf[2]), dacc, 0, 0, 0);
        dacc2 = __builtin_amdgcn_mfma_f32_16x16x32_bf16(__builtin_bit_cast(bf16x8, af[3]), __builtin_bit_cast(bf16x8, bf[3]), dacc2, 0, 0, 0);
        dacc += dacc2;
        const float n0 = dpp<XOR1>(dacc[0]), n1 = dpp<XOR1>(dacc[1]);
        if (lane < 16 && (lane & 1) == 0) {
            __builtin_amdgcn_raw_buffer_store_b128(enc4(dacc[0], dacc[1], n0, n1, tg[0], tg[1]), wsr,
                                                   WS_X2 + (u32)((j * 5 + nt) * 32 + h) * 128 + (lane >> 1) * 16, 0, 16);
        }
        STAMP(7);
    }
}

extern "C" void kernel_launch(void* const* d_in, const int* in_sizes, int n_in,
                              void* d_out, int out_size, void* d_ws, size_t ws_size,
                              hipStream_t stream) {
    fused_decode<<<256, NTHR, 0, stream>>>((const unsigned short*)d_in[0], (const u32x4*)d_in[1], (const u32x4*)d_in[2],
                                           (const u32x4*)d_in[3], (const u32x4*)d_in[4], (unsigned short*)d_out,
                                           (unsigned char*)d_ws);
}
```

```cpp
#define PROBE_W 12
#define PROBE_A 0
#define PROBE_B 1
#include <hip/hip_runtime.h>
#include <hip/hip_bf16.h>
#include <cstdint>

typedef unsigned u32;
typedef unsigned long long u64;
typedef u32 u32x4 __attribute__((ext_vector_type(4)));
typedef u32 u32x2 __attribute__((ext_vector_type(2)));
typedef float f32x4 __attribute__((ext_vector_type(4)));
typedef float f32x2 __attribute__((ext_vector_type(2)));
typedef short s16x4 __attribute__((ext_vector_type(4)));
typedef short s16x8 __attribute__((ext_vector_type(8)));
typedef __bf16 bf16x2 __attribute__((ext_vector_type(2)));
typedef __bf16 bf16x8 __attribute__((ext_vector_type(8)));
#define LAS __attribute__((address_space(3)))

constexpr int WIN = 6;
constexpr int ADA_HI = 9, ADA_LO = 4;
constexpr int ADA_T = 20;
constexpr int ADK_A = 95, ADK_B = 14;
constexpr int ADA_A = 310, ADA_B = 15;

constexpr int D = 640, H = 32, SK = 4096, DH = 128, W = 1024;
constexpr int NSW = 12, NTHR = (NSW + 1) * 64;

constexpr u32 WS_E = 0;
constexpr u32 WS_XQ = 4096;
constexpr u32 WS_X1 = WS_XQ + 32 * 8 * 1024;
constexpr u32 X1_STRIDE = 640;
constexpr u32 WS_X2 = WS_X1 + 32 * 8 * X1_STRIDE;
constexpr u32 WS_END = WS_X2 + 8 * 5 * 32 * 128;
constexpr u32 WS_STAMP = (WS_END + 4095) & ~4095u;
constexpr u32 SPIN_CAP = 1u << 20;

constexpr int L_QP = 0;
constexpr int L_OP = 4096;
constexpr int L_M = 6144;
constexpr int L_L = 6160;
constexpr int L_Q = 6208;
constexpr int L_O = 6464;
constexpr int L_TAG = 6976;
constexpr int L_FLAG = 6992;
constexpr int L_P = 7120;
constexpr int L_WO = 7632;
constexpr int L_Z = L_WO + 20480;
constexpr int L_END = L_Z + 256;
constexpr int F_QP = 0, F_Q = 1, F_OP = 2, F_O = 3, F_WO = 4, F_VGO = 5  , F_P = 9, F_KDONE = 14, F_KGO = 16  , F_WGO = 20  ;

__device__ __forceinline__ float bflo(u32 u) { return __builtin_bit_cast(float, u << 16); }
__device__ __forceinline__ float bfhi(u32 u) { return __builtin_bit_cast(float, u & 0xffff0000u); }
__device__ __forceinline__ u32 fbits(float f) { return __builtin_bit_cast(u32, f); }
__device__ __forceinline__ float bitsf(u32 u) { return __builtin_bit_cast(float, u); }
__device__ __forceinline__ u32 pack_bf16(float lo, float hi) {
    bf16x2 v = {(__bf16)lo, (__bf16)hi};
    return __builtin_bit_cast(u32, v);
}
__device__ __forceinline__ float dot2(u32 a, u32 b, float acc) {
    return __builtin_amdgcn_fdot2_f32_bf16(__builtin_bit_cast(bf16x2, a), __builtin_bit_cast(bf16x2, b), acc, false);
}
template <int CTRL> __device__ __forceinline__ float dpp(float x) {
    return __builtin_bit_cast(float, __builtin_amdgcn_mov_dpp(__builtin_bit_cast(int, x), CTRL, 0xf, 0xf, true));
}
constexpr int XOR1 = 0xB1, XOR2 = 0x4E, XOR7 = 0x141, ROR8 = 0x128, ROR4 = 0x124;
__device__ __forceinline__ float swap32_add(float a, float b) {
    auto s = __builtin_amdgcn_permlane32_swap(fbits(a), fbits(b), false, false);
    return bitsf(s[0]) + bitsf(s[1]);
}
__device__ __forceinline__ float swap16_add(float a, float b) {
    auto s = __builtin_amdgcn_permlane16_swap(fbits(a), fbits(b), false, false);
    return bitsf(s[0]) + bitsf(s[1]);
}
__device__ __forceinline__ float swap32_max(float a) {
    auto s = __builtin_amdgcn_permlane32_swap(fbits(a), fbits(a), false, false);
    return fmaxf(bitsf(s[0]), bitsf(s[1]));
}
__device__ __forceinline__ float swap16_max(float a) {
    auto s = __builtin_amdgcn_permlane16_swap(fbits(a), fbits(a), false, false);
    return fmaxf(bitsf(s[0]), bitsf(s[1]));
}
__device__ __forceinline__ float trunc24(float v) { return bitsf(fbits(v) & 0xffffff00u); }
__device__ __forceinline__ u32x4 enc4(float a, float b, float c, float d, u32 tagA, u32 tagB) {
    const u32 fa = fbits(a) + 0x80u, fb = fbits(b) + 0x80u, fc = fbits(c) + 0x80u, fd = fbits(d) + 0x80u;
    return u32x4{__builtin_amdgcn_perm(fb, fa, 0x05030201u), __builtin_amdgcn_perm(tagA, fb, 0x05040302u),
                 __builtin_amdgcn_perm(fd, fc, 0x05030201u), __builtin_amdgcn_perm(tagB, fd, 0x05040302u)};
}
__device__ __forceinline__ f32x4 dec4(u32x4 g) {
    return f32x4{bitsf(g[0] << 8), bitsf(__builtin_amdgcn_perm(g[1], g[0], 0x0504030cu)),
                 bitsf(g[2] << 8), bitsf(__builtin_amdgcn_perm(g[3], g[2], 0x0504030cu))};
}
__device__ __forceinline__ u32 badg(u32x4 g, u32 tagA, u32 tagB) { return ((g[1] >> 16) ^ tagA) | ((g[3] >> 16) ^ tagB); }

__device__ __forceinline__ void flag_add(unsigned char* lds, int f, int lane) {
    asm volatile("s_waitcnt lgkmcnt(0)" ::: "memory");
    if (lane == 0) __hip_atomic_fetch_add((LAS u32*)(lds + L_FLAG) + f, 1u, __ATOMIC_RELAXED, __HIP_MEMORY_SCOPE_WORKGROUP);
    asm volatile("" ::: "memory");
}
__device__ __forceinline__ u32 flag_add_ret(unsigned char* lds, int f, int lane) {
    asm volatile("s_waitcnt lgkmcnt(0)" ::: "memory");
    u32 old = 0;
    if (lane == 0) old = __hip_atomic_fetch_add((LAS u32*)(lds + L_FLAG) + f, 1u, __ATOMIC_RELAXED, __HIP_MEMORY_SCOPE_WORKGROUP);
    old = __builtin_amdgcn_readfirstlane(old);
    asm volatile("" ::: "memory");
    return old;
}
__device__ __forceinline__ u32 flag_get(unsigned char* lds, int f) {
    asm volatile("" ::: "memory");
    const u32 v = __hip_atomic_load((LAS u32*)(lds + L_FLAG) + f, __ATOMIC_RELAXED, __HIP_MEMORY_SCOPE_WORKGROUP);
    return __builtin_amdgcn_readfirstlane(v);
}
__device__ __forceinline__ void flag_wait(unsigned char* lds, int f, u32 want) {
#pragma nounroll
    for (u32 it = 0; it < (1u << 24); ++it) {
        if (flag_get(lds, f) >= want) break;
        __builtin_amdgcn_s_sleep(1);
    }
    asm volatile("" ::: "memory");
}
template <int N> __device__ __forceinline__ void wait_vm() {
    __builtin_amdgcn_sched_barrier(0);
    asm volatile("s_waitcnt vmcnt(%0)" :: "n"(N) : "memory");
    __builtin_amdgcn_sched_barrier(0);
}
__device__ __forceinline__ void ada_wait(u64 tstart, int target) {
    const int late = (int)(__builtin_amdgcn_s_memrealtime() - tstart) - target;
    if (late > ADA_T) wait_vm<ADA_HI - 1>(); else if (late < -ADA_T) wait_vm<ADA_LO - 1>(); else wait_vm<WIN - 1>();
}
template <int N> __device__ __forceinline__ void release_credits(unsigned char* lds, int f, int lane) {
    if constexpr (N > 0) { wait_vm<N - 1>(); flag_add(lds, f, lane); release_credits<N - 1>(lds, f, lane); }
}

#define STAMP(i) do { if ((i) == PROBE_A || (i) == PROBE_B) st[i] = __builtin_amdgcn_s_memrealtime(); } while (0)

__global__ void __launch_bounds__(NTHR) fused_decode(const unsigned short* __restrict__ x, const u32x4* __restrict__ kc,
                                                     const u32x4* __restrict__ vc, const u32x4* __restrict__ wq,
                                                     const u32x4* __restrict__ wo, unsigned short* __restrict__ out,
                                                     unsigned char* __restrict__ ws) {
    __shared__ __attribute__((aligned(16))) unsigned char lds[L_END];
    const int tid = threadIdx.x, lane = tid & 63;
    const int hw = __builtin_amdgcn_readfirstlane(tid >> 6);
    const int wv = hw == 0 ? 12 : hw <= 4 ? hw + 7 : hw - 5;
    const int g = blockIdx.x & 7, idx = blockIdx.x >> 3;
    const int h = g * 4 + (idx >> 3), j = idx & 7, b = j >> 2, s = j & 3;
    const __amdgpu_buffer_rsrc_t wsr = __builtin_amdgcn_make_buffer_rsrc(ws, 0, WS_END, 0x00020000);
    asm volatile("" :: "s"(x), "s"(kc), "s"(vc), "s"(wq), "s"(wo), "s"(out), "s"(ws));
    u64 st[8] = {0, 0, 0, 0, 0, 0, 0, 0};
    STAMP(0);
    const u64 tstart = __builtin_amdgcn_s_memrealtime();
    const int r = lane >> 4, c = lane & 15;
    const size_t kvbase = ((size_t)(b * H + h) * SK + (SK - W) + s * 256 + (wv & 3) * 64) * (DH / 8) + lane;

    if (wv < 4) {
        u32x4 kv[16];
#pragma unroll
        for (int i = 0; i < WIN - 5; ++i) kv[i] = __builtin_nontemporal_load(kc + kvbase + i * 64);
        __builtin_amdgcn_sched_barrier(0);
        __builtin_amdgcn_s_barrier();
        asm volatile("" ::: "memory");
#pragma unroll
        for (int i = WIN - 5; i < WIN; ++i) { flag_wait(lds, F_KGO + wv, i - (WIN - 5) + 1); kv[i] = __builtin_nontemporal_load(kc + kvbase + i * 64); __builtin_amdgcn_sched_barrier(0); }
        STAMP(1);
#pragma unroll
        for (int i = WIN; i < 16; ++i) { ada_wait(tstart, ADK_A + ADK_B * i); kv[i] = __builtin_nontemporal_load(kc + kvbase + i * 64); }
        __builtin_amdgcn_sched_barrier(0);
        flag_add(lds, F_KDONE, lane);
        release_credits<WIN>(lds, F_VGO + wv, lane);
        STAMP(2);
        flag_wait(lds, F_Q, 1);
        STAMP(3);
        const u32x4 q4 = *reinterpret_cast<const u32x4*>(lds + L_Q + c * 16);
        float xs[16];
#pragma unroll
        for (int i = 0; i < 16; ++i) {
            float d = 0.f;
#pragma unroll
            for (int k = 0; k < 4; ++k) d = dot2(kv[i][k], q4[k], d);
            xs[i] = d;
        }
#define TR_STEP(HALF, CTRL, BIT) _Pragma("unroll") for (int t_ = 0; t_ < (HALF); ++t_) { \
            const float lo_ = xs[t_], hi_ = xs[t_ + (HALF)]; const float t1_ = lo_ + dpp<CTRL>(lo_), t2_ = hi_ + dpp<CTRL>(hi_); \
            xs[t_] = (c & (BIT)) ? t2_ : t1_; }
        TR_STEP(8, ROR8, 8) TR_STEP(4, XOR7, 4) TR_STEP(2, XOR2, 2) TR_STEP(1, XOR1, 1)
#undef TR_STEP
        constexpr float C2 = 0.08838834764831845f * 1.4426950408889634f;
        const float sc = xs[0] * C2;
        float m = fmaxf(sc, dpp<XOR1>(sc));
        m = fmaxf(m, dpp<XOR2>(m));
        m = fmaxf(m, dpp<ROR4>(m)); m = fmaxf(m, dpp<ROR8>(m));
        m = swap16_max(m); m = swap32_max(m);
        const float p = __builtin_amdgcn_exp2f(sc - m);
        float l = p + dpp<XOR1>(p);
        l += dpp<XOR2>(l);
        l += dpp<ROR4>(l); l += dpp<ROR8>(l);
        l = swap16_add(l, l); l = swap32_add(l, l);
        const u32 ppk = pack_bf16(p, dpp<XOR1>(p));
        if ((c & 1) == 0) *reinterpret_cast<u32*>(lds + L_P + wv * 128 + r * 32 + (c >> 1) * 4) = ppk;
        if (lane == 0) { *reinterpret_cast<float*>(lds + L_M + wv * 4) = m; *reinterpret_cast<float*>(lds + L_L + wv * 4) = l; }
        flag_add(lds, F_P, lane);
        STAMP(4);
    } else if (wv < 8) {
        const int v = wv - 4;
        u32x4 vv[16];
        u32 tch = 0;
        if (v == 0) {
            const u32* p = lane == 0 ? (const u32*)(vc + kvbase) : lane == 1 ? (const u32*)(wo + (size_t)(h * DH) * (D / 8) + j * 10)
                         : lane == 2 ? (const u32*)(wo + (size_t)(h * DH + 127) * (D / 8) + j * 10) : (const u32*)out;
            if (lane < 4) tch = __builtin_nontemporal_load(p);
        }
        __builtin_amdgcn_s_barrier();
        asm volatile("" ::: "memory");
        asm volatile("" :: "v"(tch));
#pragma unroll
        for (int i = 0; i < WIN; ++i) { flag_wait(lds, F_VGO + v, i + 1); vv[i] = __builtin_nontemporal_load(vc + kvbase + i * 64); __builtin_amdgcn_sched_barrier(0); }
        STAMP(1);
#pragma unroll
        for (int i = WIN; i < 16; ++i) { ada_wait(tstart, ADA_A + ADA_B * i); vv[i] = __builtin_nontemporal_load(vc + kvbase + i * 64); }
        __builtin_amdgcn_sched_barrier(0);
        flag_add(lds, F_WGO + v, lane);
        STAMP(2);
#define VPERM(i_) _Pragma("unroll") for (int k = 0; k < 4; ++k) { \
            const u32 lo = __builtin_amdgcn_perm(vv[2 * (i_) + 1][k], vv[2 * (i_)][k], 0x05040100u); \
            const u32 hi = __builtin_amdgcn_perm(vv[2 * (i_) + 1][k], vv[2 * (i_)][k], 0x07060302u); \
            vv[2 * (i_)][k] = lo; vv[2 * (i_) + 1][k] = hi; }
#define VDOT(i_) { const u32 pp = (i_) < 4 ? pa[(i_) & 3] : pb[(i_) & 3]; _Pragma("unroll") for (int k = 0; k < 4; ++k) { \
            acc[2 * k] = dot2(vv[2 * (i_)][k], pp, acc[2 * k]); acc[2 * k + 1] = dot2(vv[2 * (i_) + 1][k], pp, acc[2 * k + 1]); } }
        VPERM(0) VPERM(1) VPERM(2) VPERM(3) VPERM(4)
#pragma unroll
        for (int i = 0; i < 10; ++i) asm volatile("" : "+v"(vv[i]));
        __builtin_amdgcn_sched_barrier(0);
        flag_wait(lds, F_P, 4);
        STAMP(3);
        const u32x4 pa = *reinterpret_cast<const u32x4*>(lds + L_P + v * 128 + r * 32);
        const u32x4 pb = *reinterpret_cast<const u32x4*>(lds + L_P + v * 128 + r * 32 + 16);
        const u32x2 tg = *reinterpret_cast<const u32x2*>(lds + L_TAG);
        const f32x4 m4 = *reinterpret_cast<const f32x4*>(lds + L_M);
        const float mown = *reinterpret_cast<const float*>(lds + L_M + v * 4);
        const float M = fmaxf(fmaxf(m4[0], m4[1]), fmaxf(m4[2], m4[3]));
        const float fv = __builtin_amdgcn_exp2f(mown - M);
        float acc[8];
#pragma unroll
        for (int k = 0; k < 8; ++k) acc[k] = 0.f;
        VDOT(0) VDOT(1) VDOT(2) VDOT(3) VDOT(4)
        __builtin_amdgcn_sched_barrier(0);
        VPERM(5) VDOT(5)
        __builtin_amdgcn_sched_barrier(0);
        VPERM(6) VDOT(6)
        __builtin_amdgcn_sched_barrier(0);
        VPERM(7) VDOT(7)
#undef VPERM
#undef VDOT
        {
            float u[4];
#pragma unroll
            for (int k = 0; k < 4; ++k) u[k] = swap32_add(acc[k], acc[k + 4]);
            f32x2 t;
            t[0] = swap16_add(u[0], u[2]) * fv; t[1] = swap16_add(u[1], u[3]) * fv;
            *reinterpret_cast<f32x2*>(lds + L_OP + v * 512 + (8 * c + 2 * r) * 4) = t;
        }
        STAMP(4);
        if (flag_add_ret(lds, F_OP, lane) == 3) {
            const int l5 = lane & 31, hf = lane >> 5;
            const f32x4 a0 = *reinterpret_cast<const f32x4*>(lds + L_OP + (2 * hf) * 512 + l5 * 16);
            const f32x4 a1 = *reinterpret_cast<const f32x4*>(lds + L_OP + (2 * hf + 1) * 512 + l5 * 16);
            const f32x4 a = a0 + a1;
            float o[4];
#pragma unroll
            for (int k = 0; k < 4; ++k) o[k] = swap32_add(a[k], a[k]);
            if (lane < 32) __builtin_amdgcn_raw_buffer_store_b128(enc4(o[0], o[1], o[2], o[3], tg[0], tg[1]), wsr,
                                                                  WS_X1 + (u32)(h * 8 + j) * X1_STRIDE + lane * 16, 0, 16);
            STAMP(5);
        }
    } else if (wv < NSW) {
        const int w4 = wv - 8;
        u32x4 wqv[5];
        const int xl = lane < 40 ? lane : 0;
        const u32x2 xv = *reinterpret_cast<const u32x2*>(x + (xl >= 20 ? D + (xl - 20) * 4 : xl * 4) + j * 80);
        __builtin_amdgcn_sched_barrier(0);
        {
            const u32x4* p = wq + (size_t)(j * 80 + w4 * 4 + r) * (H * DH / 8) + h * (DH / 8) + c;
#pragma unroll
            for (int i = 0; i < 5; ++i) wqv[i] = __builtin_nontemporal_load(p + (size_t)i * 16 * (H * DH / 8));
        }
        __builtin_amdgcn_sched_barrier(0);
        __builtin_amdgcn_s_barrier();
        asm volatile("" ::: "memory");
        release_credits<5>(lds, F_KGO + w4, lane);
        {
            float a0[8], a1[8];
#pragma unroll
            for (int k = 0; k < 8; ++k) { a0[k] = 0.f; a1[k] = 0.f; }
#pragma unroll
            for (int i = 0; i < 5; ++i) {
                const u32 xa0 = __builtin_amdgcn_readlane(xv[0], i * 4 + w4), xa1 = __builtin_amdgcn_readlane(xv[1], i * 4 + w4);
                const u32 xb0 = __builtin_amdgcn_readlane(xv[0], 20 + i * 4 + w4), xb1 = __builtin_amdgcn_readlane(xv[1], 20 + i * 4 + w4);
                const u32 sa = (r & 2) ? xa1 : xa0, sb = (r & 2) ? xb1 : xb0;
                const float x0 = (r & 1) ? bfhi(sa) : bflo(sa), x1 = (r & 1) ? bfhi(sb) : bflo(sb);
#pragma unroll
                for (int k = 0; k < 4; ++k) {
                    const float lo = bflo(wqv[i][k]), hi = bfhi(wqv[i][k]);
                    a0[2 * k] = fmaf(x0, lo, a0[2 * k]); a0[2 * k + 1] = fmaf(x0, hi, a0[2 * k + 1]);
                    a1[2 * k] = fmaf(x1, lo, a1[2 * k]); a1[2 * k + 1] = fmaf(x1, hi, a1[2 * k + 1]);
                }
            }
            float u[8];
#pragma unroll
            for (int k = 0; k < 8; ++k) u[k] = swap32_add(a0[k], a1[k]);
            f32x4 t;
#pragma unroll
            for (int k = 0; k < 4; ++k) t[k] = swap16_add(u[k], u[k + 4]);
            *reinterpret_cast<f32x4*>(lds + L_QP + w4 * 1024 + ((r >> 1) * 128 + 8 * c + 4 * (r & 1)) * 4) = t;
        }
        flag_add(lds, F_QP, lane);
        STAMP(1);
        const int t2 = w4 * 64 + lane;
        const u32x4* wop[5];
#pragma unroll
        for (int i = 0; i < 5; ++i) {
            const u32 id = i * 256 + t2, row = id / 10, cc = id - row * 10;
            wop[i] = wo + (size_t)(h * DH + row) * (D / 8) + j * 10 + cc;
        }
        flag_wait(lds, F_WGO + w4, 1);
        u32x4 wov[5];
#pragma unroll
        for (int i = 0; i < 5; ++i) wov[i] = __builtin_nontemporal_load(wop[i]);
        __builtin_amdgcn_sched_barrier(0);
        STAMP(2);
#pragma unroll
        for (int i = 0; i < 5; ++i) *reinterpret_cast<u32x4*>(lds + L_WO + (i * 256 + t2) * 16) = wov[i];
        flag_add(lds, F_WO, lane);
        STAMP(3);
    } else {
        __builtin_amdgcn_s_setprio(3);
        const u64 E = __hip_atomic_load(reinterpret_cast<u64*>(ws + WS_E), __ATOMIC_RELAXED, __HIP_MEMORY_SCOPE_AGENT);
        if (lane < 32) *reinterpret_cast<u32*>(lds + L_FLAG + lane * 4) = 0u;
        *reinterpret_cast<u32*>(lds + L_Z + lane * 4) = 0u;
        asm volatile("s_waitcnt lgkmcnt(0)" ::: "memory");
        __builtin_amdgcn_s_barrier();
        asm volatile("" ::: "memory");
        const u64 T = E + 1;
        const u32 T32 = __builtin_amdgcn_readfirstlane((u32)T);
        const u32 tagA = T32 & 0xffffu, tagB = ((T32 >> 16) + T32 * 40503u) & 0xffffu;
        if (lane == 0) *reinterpret_cast<u32x2*>(lds + L_TAG) = u32x2{tagA, tagB};
        const int l5 = lane & 31, hf = lane >> 5;
        flag_wait(lds, F_QP, 4);
        STAMP(1);
        {
            f32x4 a = *reinterpret_cast<const f32x4*>(lds + L_QP + lane * 16);
#pragma unroll
            for (int w = 1; w < 4; ++w) a += *reinterpret_cast<const f32x4*>(lds + L_QP + w * 1024 + lane * 16);
            __builtin_amdgcn_raw_buffer_store_b128(enc4(a[0], a[1], a[2], a[3], tagA, tagB), wsr, WS_XQ + (u32)(h * 8 + j) * 1024 + lane * 16, 0, 16);
        }
        u32 spins = 0;
        flag_wait(lds, F_KDONE, 4);
        {
            const u32 off = WS_XQ + (u32)(h * 8 + hf) * 1024 + b * 512 + l5 * 16;
            u32x4 gq[4];
            for (;;) {
                asm volatile("" ::: "memory");
                u32 bad = 0;
#pragma unroll
                for (int t = 0; t < 4; ++t) gq[t] = __builtin_amdgcn_raw_buffer_load_b128(wsr, off + t * 2048, 0, 16);
#pragma unroll
                for (int t = 0; t < 4; ++t) bad |= badg(gq[t], tagA, tagB);
                if (!__any(bad != 0) || ++spins > SPIN_CAP) break;
            }
            const f32x4 qs = (dec4(gq[0]) + dec4(gq[1])) + (dec4(gq[2]) + dec4(gq[3]));
            const float q0 = swap32_add(qs[0], qs[2]), q1 = swap32_add(qs[1], qs[3]);
            *reinterpret_cast<u32*>(lds + L_Q + l5 * 8 + hf * 4) = pack_bf16(q0, q1);
            flag_add(lds, F_Q, lane);
        }
        STAMP(2);
        {
            const u32x4 wrm = __builtin_amdgcn_raw_buffer_load_b128(wsr, WS_X1 + (u32)(h * 8 + j) * X1_STRIDE + l5 * 16, 0, 16);
            asm volatile("" :: "v"(wrm));
        }
        flag_wait(lds, F_P, 4);
        {
            const f32x4 m4 = *reinterpret_cast<const f32x4*>(lds + L_M);
            const f32x4 l4 = *reinterpret_cast<const f32x4*>(lds + L_L);
            const float M = fmaxf(fmaxf(m4[0], m4[1]), fmaxf(m4[2], m4[3]));
            const float Mhi = trunc24(M), Mlo = M - Mhi;
            float L = 0.f;
#pragma unroll
            for (int w = 0; w < 4; ++w) L = fmaf(__builtin_amdgcn_exp2f(m4[w] - M), l4[w], L);
            if (lane < 8) __builtin_amdgcn_raw_buffer_store_b128(enc4(Mhi, L, Mlo, L, tagA, tagB), wsr,
                                                                 WS_X1 + (u32)(h * 8 + j) * X1_STRIDE + 512 + lane * 16, 0, 16);
        }
        STAMP(3);
        flag_wait(lds, F_OP, 4);
        STAMP(4);
        {
            const u32 off = WS_X1 + (u32)(h * 8) * X1_STRIDE;
            u32x4 go[4], gm;
            for (;;) {
                asm volatile("" ::: "memory");
                gm = __builtin_amdgcn_raw_buffer_load_b128(wsr, off + (lane & 7) * X1_STRIDE + 512, 0, 16);
#pragma unroll
                for (int t = 0; t < 4; ++t) go[t] = __builtin_amdgcn_raw_buffer_load_b128(wsr, off + (2 * t + hf) * X1_STRIDE + l5 * 16, 0, 16);
                u32 bad = badg(gm, tagA, tagB);
#pragma unroll
                for (int t = 0; t < 4; ++t) bad |= badg(go[t], tagA, tagB);
                if (!__any(bad != 0) || ++spins > SPIN_CAP) break;
            }
            const f32x4 ml = dec4(gm);
            const float ms = ml[0] + ml[2];
            float M = fmaxf(ms, dpp<XOR1>(ms)); M = fmaxf(M, dpp<XOR2>(M));
            const float f = __builtin_amdgcn_exp2f(ms - M);
            float L = f * ml[1]; L += dpp<XOR1>(L); L += dpp<XOR2>(L);
            const float fw = f * __builtin_amdgcn_rcpf(L);
            float wt[4];
#pragma unroll
            for (int t = 0; t < 4; ++t) {
                const float w0 = bitsf(__builtin_amdgcn_readlane(fbits(fw), 2 * t)), w1 = bitsf(__builtin_amdgcn_readlane(fbits(fw), 2 * t + 1));
                wt[t] = hf ? w1 : w0;
            }
            const f32x4 ob0 = dec4(go[0]) * wt[0] + dec4(go[1]) * wt[1];
            const f32x4 ob1 = dec4(go[2]) * wt[2] + dec4(go[3]) * wt[3];
            float o[4];
#pragma unroll
            for (int k = 0; k < 4; ++k) o[k] = swap32_add(ob0[k], ob1[k]);
            *reinterpret_cast<u32x2*>(lds + L_O + lane * 8) = u32x2{pack_bf16(o[0], o[1]), pack_bf16(o[2], o[3])};
            flag_add(lds, F_O, lane);
        }
        STAMP(5);
        const int rho = h / 6;
        if (h == rho * 6 && rho < 5) {
            const u32 off = WS_X2 + (u32)((j * 5 + rho) * 32) * 128 + lane * 16;
            u32x4 g0, g1, g2, g3;
            for (;;) {
                asm volatile("" ::: "memory");
                g0 = __builtin_amdgcn_raw_buffer_load_b128(wsr, off, 0, 16);
                g1 = __builtin_amdgcn_raw_buffer_load_b128(wsr, off + 1024, 0, 16);
                g2 = __builtin_amdgcn_raw_buffer_load_b128(wsr, off + 2048, 0, 16);
                g3 = __builtin_amdgcn_raw_buffer_load_b128(wsr, off + 3072, 0, 16);
                const u32 bad = badg(g0, tagA, tagB) | badg(g1, tagA, tagB) | badg(g2, tagA, tagB) | badg(g3, tagA, tagB);
                if (!__any(bad != 0)) break;
                if (++spins > SPIN_CAP) break;
            }
            STAMP(6);
            const f32x4 y4 = (dec4(g0) + dec4(g1)) + (dec4(g2) + dec4(g3));
            const float ya = swap32_add(y4[0], y4[1]), yb = swap32_add(y4[2], y4[3]);
            float y = swap16_add(ya, yb);
            y += dpp<ROR8>(y);
            if ((lane & 8) == 0) {
                const __bf16 ybf = (__bf16)y;
                out[hf * D + j * 80 + 16 * rho + 2 * (lane & 7) + ((lane >> 4) & 1)] = __builtin_bit_cast(unsigned short, ybf);
            }
            if (blockIdx.x == 0 && lane == 0) __hip_atomic_store(reinterpret_cast<u64*>(ws + WS_E), T, __ATOMIC_RELAXED, __HIP_MEMORY_SCOPE_AGENT);
        }
        STAMP(7);
    }
    if (wv < 4 || wv == 8) {
        const int nt = wv < 4 ? wv : 4;
        flag_wait(lds, F_WO, 4);
        const int q_ = (lane & 15) >> 2, p_ = lane & 3;
        const u32 tb = L_WO + (u32)(8 * r + q_) * 160 + 32 * nt + 8 * p_;
        s16x8 bf[4];
#pragma unroll
        for (int t = 0; t < 4; ++t) {
            const s16x4 b0 = __builtin_amdgcn_ds_read_tr16_b64_v4i16((LAS s16x4*)(lds + tb + t * 32 * 160));
            const s16x4 b1 = __builtin_amdgcn_ds_read_tr16_b64_v4i16((LAS s16x4*)(lds + tb + t * 32 * 160 + 4 * 160));
            bf[t] = s16x8{b0[0], b0[1], b0[2], b0[3], b1[0], b1[1], b1[2], b1[3]};
        }
        const u32x2 tg = *reinterpret_cast<const u32x2*>(lds + L_TAG);
        asm volatile("" : "+v"(bf[0]), "+v"(bf[1]), "+v"(bf[2]), "+v"(bf[3]));
        const u32 ab = (c < 2 ? L_O + c * 256 : L_Z) + 16 * r;
        flag_wait(lds, F_O, 1);
        STAMP(6);
        f32x4 dacc = {0.f, 0.f, 0.f, 0.f}, dacc2 = {0.f, 0.f, 0.f, 0.f};
        u32x4 af[4];
#pragma unroll
        for (int t = 0; t < 4; ++t) af[t] = *reinterpret_cast<const u32x4*>(lds + ab + 64 * t);
        dacc = __builtin_amdgcn_mfma_f32_16x16x32_bf16(__builtin_bit_cast(bf16x8, af[0]), __builtin_bit_cast(bf16x8, bf[0]), dacc, 0, 0, 0);
        dacc2 = __builtin_amdgcn_mfma_f32_16x16x32_bf16(__builtin_bit_cast(bf16x8, af[1]), __builtin_bit_cast(bf16x8, bf[1]), dacc2, 0, 0, 0);
        dacc = __builtin_amdgcn_mfma_f32_16x16x32_bf16(__builtin_bit_cast(bf16x8, af[2]), __builtin_bit_cast(bf16x8, bf[2]), dacc, 0, 0, 0);
        dacc2 = __builtin_amdgcn_mfma_f32_16x16x32_bf16(__builtin_bit_cast(bf16x8, af[3]), __builtin_bit_cast(bf16x8, bf[3]), dacc2, 0, 0, 0);
        dacc += dacc2;
        const float n0 = dpp<XOR1>(dacc[0]), n1 = dpp<XOR1>(dacc[1]);
        if (lane < 16 && (lane & 1) == 0) {
            __builtin_amdgcn_raw_buffer_store_b128(enc4(dacc[0], dacc[1], n0, n1, tg[0], tg[1]), wsr,
                                                   WS_X2 + (u32)((j * 5 + nt) * 32 + h) * 128 + (lane >> 1) * 16, 0, 16);
        }
        STAMP(7);
    }
    if (blockIdx.x == 0 && wv == PROBE_W) {
        const u64 dl = (st[PROBE_B] - st[PROBE_A]) * 8;
        const u64 lim = dl < 20000 ? dl : 20000;
        while (__builtin_amdgcn_s_memrealtime() - st[0] < 2000 + lim) __builtin_amdgcn_s_sleep(2);
    }
}

extern "C" void kernel_launch(void* const* d_in, const int* in_sizes, int n_in,
                              void* d_out, int out_size, void* d_ws, size_t ws_size,
                              hipStream_t stream) {
    fused_decode<<<256, NTHR, 0, stream>>>((const unsigned short*)d_in[0], (const u32x4*)d_in[1], (const u32x4*)d_in[2],
                                           (const u32x4*)d_in[3], (const u32x4*)d_in[4], (unsigned short*)d_out,
                                           (unsigned char*)d_ws);
}
```

```cpp
#include <hip/hip_runtime.h>
#include <hip/hip_bf16.h>
#include <cstdint>

typedef unsigned u32;
typedef unsigned long long u64;
typedef u32 u32x4 __attribute__((ext_vector_type(4)));
typedef u32 u32x2 __attribute__((ext_vector_type(2)));
typedef float f32x4 __attribute__((ext_vector_type(4)));
typedef float f32x2 __attribute__((ext_vector_type(2)));
typedef short s16x4 __attribute__((ext_vector_type(4)));
typedef short s16x8 __attribute__((ext_vector_type(8)));
typedef __bf16 bf16x2 __attribute__((ext_vector_type(2)));
typedef __bf16 bf16x8 __attribute__((ext_vector_type(8)));
#define LAS __attribute__((address_space(3)))

constexpr int WIN = 6;
constexpr int ADA_HI = 9, ADA_LO = 4;
constexpr int ADA_T = 20;
constexpr int ADK_A = 95, ADK_B = 14;
constexpr int ADA_A = 310, ADA_B = 15;

constexpr int D = 640, H = 32, SK = 4096, DH = 128, W = 1024;
constexpr int NSW = 12, NTHR = (NSW + 1) * 64;

constexpr u32 WS_E = 0;
constexpr u32 WS_XQ = 4096;
constexpr u32 WS_X1 = WS_XQ + 32 * 8 * 1024;
constexpr u32 X1_STRIDE = 640;
constexpr u32 WS_X2 = WS_X1 + 32 * 8 * X1_STRIDE;
constexpr u32 WS_END = WS_X2 + 8 * 5 * 32 * 128;
constexpr u32 WS_STAMP = (WS_END + 4095) & ~4095u;
constexpr u32 SPIN_CAP = 1u << 20;

constexpr int L_QP = 0;
constexpr int L_OP = 4096;
constexpr int L_M = 6144;
constexpr int L_L = 6160;
constexpr int L_Q = 6208;
constexpr int L_O = 6464;
constexpr int L_TAG = 6976;
constexpr int L_FLAG = 6992;
constexpr int L_P = 7120;
constexpr int L_WO = 7632;
constexpr int L_Z = L_WO + 20480;
constexpr int L_END = L_Z + 256;
constexpr int F_QP = 0, F_Q = 1, F_OP = 2, F_O = 3, F_WO = 4, F_VGO = 5  , F_P = 9, F_KDONE = 14, F_KGO = 16  , F_WGO = 20  ;

__device__ __forceinline__ float bflo(u32 u) { return __builtin_bit_cast(float, u << 16); }
__device__ __forceinline__ float bfhi(u32 u) { return __builtin_bit_cast(float, u & 0xffff0000u); }
__device__ __forceinline__ u32 fbits(float f) { return __builtin_bit_cast(u32, f); }
__device__ __forceinline__ float bitsf(u32 u) { return __builtin_bit_cast(float, u); }
__device__ __forceinline__ u32 pack_bf16(float lo, float hi) {
    bf16x2 v = {(__bf16)lo, (__bf16)hi};
    return __builtin_bit_cast(u32, v);
}
__device__ __forceinline__ float dot2(u32 a, u32 b, float acc) {
    return __builtin_amdgcn_fdot2_f32_bf16(__builtin_bit_cast(bf16x2, a), __builtin_bit_cast(bf16x2, b), acc, false);
}
template <int CTRL> __device__ __forceinline__ float dpp(float x) {
    return __builtin_bit_cast(float, __builtin_amdgcn_mov_dpp(__builtin_bit_cast(int, x), CTRL, 0xf, 0xf, true));
}
constexpr int XOR1 = 0xB1, XOR2 = 0x4E, XOR7 = 0x141, ROR8 = 0x128, ROR4 = 0x124;
__device__ __forceinline__ float swap32_add(float a, float b) {
    auto s = __builtin_amdgcn_permlane32_swap(fbits(a), fbits(b), false, false);
    return bitsf(s[0]) + bitsf(s[1]);
}
__device__ __forceinline__ float swap16_add(float a, float b) {
    auto s = __builtin_amdgcn_permlane16_swap(fbits(a), fbits(b), false, false);
    return bitsf(s[0]) + bitsf(s[1]);
}
__device__ __forceinline__ float swap32_max(float a) {
    auto s = __builtin_amdgcn_permlane32_swap(fbits(a), fbits(a), false, false);
    return fmaxf(bitsf(s[0]), bitsf(s[1]));
}
__device__ __forceinline__ float swap16_max(float a) {
    auto s = __builtin_amdgcn_permlane16_swap(fbits(a), fbits(a), false, false);
    return fmaxf(bitsf(s[0]), bitsf(s[1]));
}
__device__ __forceinline__ float trunc24(float v) { return bitsf(fbits(v) & 0xffffff00u); }
__device__ __forceinline__ u32x4 enc4(float a, float b, float c, float d, u32 tagA, u32 tagB) {
    const u32 fa = fbits(a) + 0x80u, fb = fbits(b) + 0x80u, fc = fbits(c) + 0x80u, fd = fbits(d) + 0x80u;
    return u32x4{__builtin_amdgcn_perm(fb, fa, 0x05030201u), __builtin_amdgcn_perm(tagA, fb, 0x05040302u),
                 __builtin_amdgcn_perm(fd, fc, 0x05030201u), __builtin_amdgcn_perm(tagB, fd, 0x05040302u)};
}
__device__ __forceinline__ f32x4 dec4(u32x4 g) {
    return f32x4{bitsf(g[0] << 8), bitsf(__builtin_amdgcn_perm(g[1], g[0], 0x0504030cu)),
                 bitsf(g[2] << 8), bitsf(__builtin_amdgcn_perm(g[3], g[2], 0x0504030cu))};
}
__device__ __forceinline__ u32 badg(u32x4 g, u32 tagA, u32 tagB) { return ((g[1] >> 16) ^ tagA) | ((g[3] >> 16) ^ tagB); }

__device__ __forceinline__ void flag_add(unsigned char* lds, int f, int lane) {
    asm volatile("s_waitcnt lgkmcnt(0)" ::: "memory");
    if (lane == 0) __hip_atomic_fetch_add((LAS u32*)(lds + L_FLAG) + f, 1u, __ATOMIC_RELAXED, __HIP_MEMORY_SCOPE_WORKGROUP);
    asm volatile("" ::: "memory");
}
__device__ __forceinline__ u32 flag_get(unsigned char* lds, int f) {
    asm volatile("" ::: "memory");
    const u32 v = __hip_atomic_load((LAS u32*)(lds + L_FLAG) + f, __ATOMIC_RELAXED, __HIP_MEMORY_SCOPE_WORKGROUP);
    return __builtin_amdgcn_readfirstlane(v);
}
__device__ __forceinline__ void flag_wait(unsigned char* lds, int f, u32 want) {
#pragma nounroll
    for (u32 it = 0; it < (1u << 24); ++it) {
        if (flag_get(lds, f) >= want) break;
        __builtin_amdgcn_s_sleep(1);
    }
    asm volatile("" ::: "memory");
}
template <int N> __device__ __forceinline__ void wait_vm() {
    __builtin_amdgcn_sched_barrier(0);
    asm volatile("s_waitcnt vmcnt(%0)" :: "n"(N) : "memory");
    __builtin_amdgcn_sched_barrier(0);
}
__device__ __forceinline__ void ada_wait(u64 tstart, int target) {
    const int late = (int)(__builtin_amdgcn_s_memrealtime() - tstart) - target;
    if (late > ADA_T) wait_vm<ADA_HI - 1>(); else if (late < -ADA_T) wait_vm<ADA_LO - 1>(); else wait_vm<WIN - 1>();
}
template <int N> __device__ __forceinline__ void release_credits(unsigned char* lds, int f, int lane) {
    if constexpr (N > 0) { wait_vm<N - 1>(); flag_add(lds, f, lane); release_credits<N - 1>(lds, f, lane); }
}

#define STAMP(i) do { } while (0)

__global__ void __launch_bounds__(NTHR, 4) fused_decode(const unsigned short* __restrict__ x, const u32x4* __restrict__ kc,
                                                     const u32x4* __restrict__ vc, const u32x4* __restrict__ wq,
                                                     const u32x4* __restrict__ wo, unsigned short* __restrict__ out,
                                                     unsigned char* __restrict__ ws) {
    __shared__ __attribute__((aligned(16))) unsigned char lds[L_END];
    const int tid = threadIdx.x, lane = tid & 63;
    const int hw = __builtin_amdgcn_readfirstlane(tid >> 6);
    const int wv = hw == 0 ? 12 : hw <= 4 ? hw + 7 : hw - 5;
    const int g = blockIdx.x & 7, idx = blockIdx.x >> 3;
    const int h = g * 4 + (idx >> 3), j = idx & 7, b = j >> 2, s = j & 3;
    const __amdgpu_buffer_rsrc_t wsr = __builtin_amdgcn_make_buffer_rsrc(ws, 0, WS_END, 0x00020000);
    asm volatile("" :: "s"(x), "s"(kc), "s"(vc), "s"(wq), "s"(wo), "s"(out), "s"(ws));
    STAMP(0);
    const u64 tstart = __builtin_amdgcn_s_memrealtime();
    const int r = lane >> 4, c = lane & 15;
    const size_t kvbase = ((size_t)(b * H + h) * SK + (SK - W) + s * 256 + (wv & 3) * 64) * (DH / 8) + lane;

    if (wv < 4) {
        u32x4 kv[16];
#pragma unroll
        for (int i = 0; i < WIN - 5; ++i) kv[i] = __builtin_nontemporal_load(kc + kvbase + i * 64);
        __builtin_amdgcn_sched_barrier(0);
        __builtin_amdgcn_s_barrier();
        asm volatile("" ::: "memory");
#pragma unroll
        for (int i = WIN - 5; i < WIN; ++i) { flag_wait(lds, F_KGO + wv, i - (WIN - 5) + 1); kv[i] = __builtin_nontemporal_load(kc + kvbase + i * 64); __builtin_amdgcn_sched_barrier(0); }
        STAMP(1);
#pragma unroll
        for (int i = WIN; i < 16; ++i) { ada_wait(tstart, ADK_A + ADK_B * i); kv[i] = __builtin_nontemporal_load(kc + kvbase + i * 64); }
        __builtin_amdgcn_sched_barrier(0);
        flag_add(lds, F_KDONE, lane);
        release_credits<WIN>(lds, F_VGO + wv, lane);
        STAMP(2);
        flag_wait(lds, F_Q, 1);
        STAMP(3);
        const u32x4 q4 = *reinterpret_cast<const u32x4*>(lds + L_Q + c * 16);
        float xs[16];
#pragma unroll
        for (int i = 0; i < 16; ++i) {
            float d = 0.f;
#pragma unroll
            for (int k = 0; k < 4; ++k) d = dot2(kv[i][k], q4[k], d);
            xs[i] = d;
        }
#define TR_STEP(HALF, CTRL, BIT) _Pragma("unroll") for (int t_ = 0; t_ < (HALF); ++t_) { \
            const float lo_ = xs[t_], hi_ = xs[t_ + (HALF)]; const float t1_ = lo_ + dpp<CTRL>(lo_), t2_ = hi_ + dpp<CTRL>(hi_); \
            xs[t_] = (c & (BIT)) ? t2_ : t1_; }
        TR_STEP(8, ROR8, 8) TR_STEP(4, XOR7, 4) TR_STEP(2, XOR2, 2) TR_STEP(1, XOR1, 1)
#undef TR_STEP
        constexpr float C2 = 0.08838834764831845f * 1.4426950408889634f;
        const float sc = xs[0] * C2;
        float m = fmaxf(sc, dpp<XOR1>(sc));
        m = fmaxf(m, dpp<XOR2>(m));
        m = fmaxf(m, dpp<ROR4>(m)); m = fmaxf(m, dpp<ROR8>(m));
        m = swap16_max(m); m = swap32_max(m);
        const float p = __builtin_amdgcn_exp2f(sc - m);
        float l = p + dpp<XOR1>(p);
        l += dpp<XOR2>(l);
        l += dpp<ROR4>(l); l += dpp<ROR8>(l);
        l = swap16_add(l, l); l = swap32_add(l, l);
        const u32 ppk = pack_bf16(p, dpp<XOR1>(p));
        if ((c & 1) == 0) *reinterpret_cast<u32*>(lds + L_P + wv * 128 + r * 32 + (c >> 1) * 4) = ppk;
        if (lane == 0) { *reinterpret_cast<float*>(lds + L_M + wv * 4) = m; *reinterpret_cast<float*>(lds + L_L + wv * 4) = l; }
        flag_add(lds, F_P, lane);
        STAMP(4);
    } else if (wv < 8) {
        const int v = wv - 4;
        u32x4 vv[16];
        u32 tch = 0;
        if (v == 0) {
            const u32* p = lane == 0 ? (const u32*)(vc + kvbase) : lane == 1 ? (const u32*)(wo + (size_t)(h * DH) * (D / 8) + j * 10)
                         : (const u32*)(wo + (size_t)(h * DH + 127) * (D / 8) + j * 10);
            if (lane < 3) tch = __builtin_nontemporal_load(p);
        }
        __builtin_amdgcn_s_barrier();
        asm volatile("" ::: "memory");
        asm volatile("" :: "v"(tch));
#pragma unroll
        for (int i = 0; i < WIN; ++i) { flag_wait(lds, F_VGO + v, i + 1); vv[i] = __builtin_nontemporal_load(vc + kvbase + i * 64); __builtin_amdgcn_sched_barrier(0); }
        STAMP(1);
#pragma unroll
        for (int i = WIN; i < 16; ++i) { ada_wait(tstart, ADA_A + ADA_B * i); vv[i] = __builtin_nontemporal_load(vc + kvbase + i * 64); }
        __builtin_amdgcn_sched_barrier(0);
        flag_add(lds, F_WGO + v, lane);
        STAMP(2);
#define VPERM(i_) _Pragma("unroll") for (int k = 0; k < 4; ++k) { \
            const u32 lo = __builtin_amdgcn_perm(vv[2 * (i_) + 1][k], vv[2 * (i_)][k], 0x05040100u); \
            const u32 hi = __builtin_amdgcn_perm(vv[2 * (i_) + 1][k], vv[2 * (i_)][k], 0x07060302u); \
            vv[2 * (i_)][k] = lo; vv[2 * (i_) + 1][k] = hi; }
#define VDOT(i_) { const u32 pp = (i_) < 4 ? pa[(i_) & 3] : pb[(i_) & 3]; _Pragma("unroll") for (int k = 0; k < 4; ++k) { \
            acc[2 * k] = dot2(vv[2 * (i_)][k], pp, acc[2 * k]); acc[2 * k + 1] = dot2(vv[2 * (i_) + 1][k], pp, acc[2 * k + 1]); } }
        VPERM(0) VPERM(1) VPERM(2) VPERM(3) VPERM(4)
#pragma unroll
        for (int i = 0; i < 10; ++i) asm volatile("" : "+v"(vv[i]));
        __builtin_amdgcn_sched_barrier(0);
        flag_wait(lds, F_P, 4);
        STAMP(3);
        const u32x4 pa = *reinterpret_cast<const u32x4*>(lds + L_P + v * 128 + r * 32);
        const u32x4 pb = *reinterpret_cast<const u32x4*>(lds + L_P + v * 128 + r * 32 + 16);
        const u32x2 tg = *reinterpret_cast<const u32x2*>(lds + L_TAG);
        const f32x4 m4 = *reinterpret_cast<const f32x4*>(lds + L_M);
        const float mown = *reinterpret_cast<const float*>(lds + L_M + v * 4);
        const float M = fmaxf(fmaxf(m4[0], m4[1]), fmaxf(m4[2], m4[3]));
        const float fv = __builtin_amdgcn_exp2f(mown - M);
        float acc[8];
#pragma unroll
        for (int k = 0; k < 8; ++k) acc[k] = 0.f;
        VDOT(0) VDOT(1) VDOT(2) VDOT(3) VDOT(4)
        __builtin_amdgcn_sched_barrier(0);
        VPERM(5) VDOT(5)
        __builtin_amdgcn_sched_barrier(0);
        VPERM(6) VDOT(6)
        __builtin_amdgcn_sched_barrier(0);
        VPERM(7) VDOT(7)
#undef VPERM
#undef VDOT
        {
            float u[4];
#pragma unroll
            for (int k = 0; k < 4; ++k) u[k] = swap32_add(acc[k], acc[k + 4]);
            f32x2 t;
            t[0] = swap16_add(u[0], u[2]) * fv; t[1] = swap16_add(u[1], u[3]) * fv;
            *reinterpret_cast<f32x2*>(lds + L_OP + v * 512 + (8 * c + 2 * r) * 4) = t;
        }
        STAMP(4);
        const int l5 = lane & 31, hf = lane >> 5;
        u32 old;
        f32x4 a0, a1;
        const u32 lbase = (u32)(size_t)(LAS unsigned char*)lds;
        asm volatile("s_mov_b64 exec, 1\n\t"
                     "ds_add_rtn_u32 %0, %3, %4\n\t"
                     "s_mov_b64 exec, -1\n\t"
                     "ds_read_b128 %1, %5\n\t"
                     "ds_read_b128 %2, %5 offset:512\n\t"
                     "s_waitcnt lgkmcnt(0)"
                     : "=&v"(old), "=&v"(a0), "=&v"(a1)
                     : "v"(lbase + L_FLAG + F_OP * 4), "v"(1u), "v"(lbase + L_OP + (2 * hf) * 512 + l5 * 16) : "memory");
        if (__builtin_amdgcn_readfirstlane(old) == 3) {
            const f32x4 a = a0 + a1;
            float o[4];
#pragma unroll
            for (int k = 0; k < 4; ++k) o[k] = swap32_add(a[k], a[k]);
            if (lane < 32) __builtin_amdgcn_raw_buffer_store_b128(enc4(o[0], o[1], o[2], o[3], tg[0], tg[1]), wsr,
                                                                  WS_X1 + (u32)(h * 8 + j) * X1_STRIDE + lane * 16, 0, 16);
            STAMP(5);
        }
    } else if (wv < NSW) {
        const int w4 = wv - 8;
        u32x4 wqv[5];
        const int xl = lane < 40 ? lane : 0;
        const u32x2 xv = *reinterpret_cast<const u32x2*>(x + (xl >= 20 ? D + (xl - 20) * 4 : xl * 4) + j * 80);
        __builtin_amdgcn_sched_barrier(0);
        {
            const u32x4* p = wq + (size_t)(j * 80 + w4 * 4 + r) * (H * DH / 8) + h * (DH / 8) + c;
#pragma unroll
            for (int i = 0; i < 5; ++i) wqv[i] = __builtin_nontemporal_load(p + (size_t)i * 16 * (H * DH / 8));
        }
        __builtin_amdgcn_sched_barrier(0);
        __builtin_amdgcn_s_barrier();
        asm volatile("" ::: "memory");
        release_credits<5>(lds, F_KGO + w4, lane);
        {
            float a0[8], a1[8];
#pragma unroll
            for (int k = 0; k < 8; ++k) { a0[k] = 0.f; a1[k] = 0.f; }
#pragma unroll
            for (int i = 0; i < 5; ++i) {
                const u32 xa0 = __builtin_amdgcn_readlane(xv[0], i * 4 + w4), xa1 = __builtin_amdgcn_readlane(xv[1], i * 4 + w4);
                const u32 xb0 = __builtin_amdgcn_readlane(xv[0], 20 + i * 4 + w4), xb1 = __builtin_amdgcn_readlane(xv[1], 20 + i * 4 + w4);
                const u32 sa = (r & 2) ? xa1 : xa0, sb = (r & 2) ? xb1 : xb0;
                const float x0 = (r & 1) ? bfhi(sa) : bflo(sa), x1 = (r & 1) ? bfhi(sb) : bflo(sb);
#pragma unroll
                for (int k = 0; k < 4; ++k) {
                    const float lo = bflo(wqv[i][k]), hi = bfhi(wqv[i][k]);
                    a0[2 * k] = fmaf(x0, lo, a0[2 * k]); a0[2 * k + 1] = fmaf(x0, hi, a0[2 * k + 1]);
                    a1[2 * k] = fmaf(x1, lo, a1[2 * k]); a1[2 * k + 1] = fmaf(x1, hi, a1[2 * k + 1]);
                }
            }
            float u[8];
#pragma unroll
            for (int k = 0; k < 8; ++k) u[k] = swap32_add(a0[k], a1[k]);
            f32x4 t;
#pragma unroll
            for (int k = 0; k < 4; ++k) t[k] = swap16_add(u[k], u[k + 4]);
            *reinterpret_cast<f32x4*>(lds + L_QP + w4 * 1024 + ((r >> 1) * 128 + 8 * c + 4 * (r & 1)) * 4) = t;
        }
        flag_add(lds, F_QP, lane);
        STAMP(1);
        const int t2 = w4 * 64 + lane;
        const u32x4* wop[5];
#pragma unroll
        for (int i = 0; i < 5; ++i) {
            const u32 id = i * 256 + t2, row = id / 10, cc = id - row * 10;
            wop[i] = wo + (size_t)(h * DH + row) * (D / 8) + j * 10 + cc;
        }
        flag_wait(lds, F_WGO + w4, 1);
        u32x4 wov[5];
#pragma unroll
        for (int i = 0; i < 5; ++i) wov[i] = __builtin_nontemporal_load(wop[i]);
        __builtin_amdgcn_sched_barrier(0);
        STAMP(2);
#pragma unroll
        for (int i = 0; i < 5; ++i) *reinterpret_cast<u32x4*>(lds + L_WO + (i * 256 + t2) * 16) = wov[i];
        flag_add(lds, F_WO, lane);
        STAMP(3);
    } else {
        __builtin_amdgcn_s_setprio(3);
        const u64 E = __hip_atomic_load(reinterpret_cast<u64*>(ws + WS_E), __ATOMIC_RELAXED, __HIP_MEMORY_SCOPE_AGENT);
        if (lane < 32) *reinterpret_cast<u32*>(lds + L_FLAG + lane * 4) = 0u;
        *reinterpret_cast<u32*>(lds + L_Z + lane * 4) = 0u;
        asm volatile("s_waitcnt lgkmcnt(0)" ::: "memory");
        __builtin_amdgcn_s_barrier();
        asm volatile("" ::: "memory");
        const u64 T = E + 1;
        const u32 T32 = __builtin_amdgcn_readfirstlane((u32)T);
        const u32 tagA = T32 & 0xffffu, tagB = ((T32 >> 16) + T32 * 40503u) & 0xffffu;
        if (lane == 0) *reinterpret_cast<u32x2*>(lds + L_TAG) = u32x2{tagA, tagB};
        const int l5 = lane & 31, hf = lane >> 5;
        flag_wait(lds, F_QP, 4);
        STAMP(1);
        {
            f32x4 a = *reinterpret_cast<const f32x4*>(lds + L_QP + lane * 16);
#pragma unroll
            for (int w = 1; w < 4; ++w) a += *reinterpret_cast<const f32x4*>(lds + L_QP + w * 1024 + lane * 16);
            __builtin_amdgcn_raw_buffer_store_b128(enc4(a[0], a[1], a[2], a[3], tagA, tagB), wsr, WS_XQ + (u32)(h * 8 + j) * 1024 + lane * 16, 0, 16);
        }
        u32 spins = 0;
        flag_wait(lds, F_KDONE, 4);
        {
            const u32 off = WS_XQ + (u32)(h * 8 + hf) * 1024 + b * 512 + l5 * 16;
            u32x4 gq[4];
            for (;;) {
                asm volatile("" ::: "memory");
                u32 bad = 0;
#pragma unroll
                for (int t = 0; t < 4; ++t) gq[t] = __builtin_amdgcn_raw_buffer_load_b128(wsr, off + t * 2048, 0, 16);
#pragma unroll
                for (int t = 0; t < 4; ++t) bad |= badg(gq[t], tagA, tagB);
                if (!__any(bad != 0) || ++spins > SPIN_CAP) break;
            }
            const f32x4 qs = (dec4(gq[0]) + dec4(gq[1])) + (dec4(gq[2]) + dec4(gq[3]));
            const float q0 = swap32_add(qs[0], qs[2]), q1 = swap32_add(qs[1], qs[3]);
            *reinterpret_cast<u32*>(lds + L_Q + l5 * 8 + hf * 4) = pack_bf16(q0, q1);
            flag_add(lds, F_Q, lane);
        }
        STAMP(2);
        {
            const u32x4 wrm = __builtin_amdgcn_raw_buffer_load_b128(wsr, WS_X1 + (u32)(h * 8 + j) * X1_STRIDE + l5 * 16, 0, 16);
            asm volatile("" :: "v"(wrm));
        }
        flag_wait(lds, F_P, 4);
        {
            const f32x4 m4 = *reinterpret_cast<const f32x4*>(lds + L_M);
            const f32x4 l4 = *reinterpret_cast<const f32x4*>(lds + L_L);
            const float M = fmaxf(fmaxf(m4[0], m4[1]), fmaxf(m4[2], m4[3]));
            const float Mhi = trunc24(M), Mlo = M - Mhi;
            float L = 0.f;
#pragma unroll
            for (int w = 0; w < 4; ++w) L = fmaf(__builtin_amdgcn_exp2f(m4[w] - M), l4[w], L);
            if (lane < 8) __builtin_amdgcn_raw_buffer_store_b128(enc4(Mhi, L, Mlo, L, tagA, tagB), wsr,
                                                                 WS_X1 + (u32)(h * 8 + j) * X1_STRIDE + 512 + lane * 16, 0, 16);
        }
        STAMP(3);
        flag_wait(lds, F_OP, 4);
        STAMP(4);
        {
            const u32 off = WS_X1 + (u32)(h * 8) * X1_STRIDE;
            u32x4 go[4];
            float wt[4] = {0.f, 0.f, 0.f, 0.f};
            bool have = false;
            for (;;) {
                asm volatile("" ::: "memory");
                u32x4 gm = {0u, 0u, 0u, 0u};
                if (!have) gm = __builtin_amdgcn_raw_buffer_load_b128(wsr, off + (lane & 7) * X1_STRIDE + 512 + (lane >> 3) * 16, 0, 16);
#pragma unroll
                for (int t = 0; t < 4; ++t) go[t] = __builtin_amdgcn_raw_buffer_load_b128(wsr, off + (2 * t + hf) * X1_STRIDE + l5 * 16, 0, 16);
                asm volatile("" ::: "memory");
                if (!have && !__any(badg(gm, tagA, tagB) != 0)) {
                    const f32x4 ml = dec4(gm);
                    const float ms = ml[0] + ml[2];
                    float M = fmaxf(ms, dpp<XOR1>(ms)); M = fmaxf(M, dpp<XOR2>(M));
                    const float f = __builtin_amdgcn_exp2f(ms - M);
                    float L = f * ml[1]; L += dpp<XOR1>(L); L += dpp<XOR2>(L);
                    const float fw = f * __builtin_amdgcn_rcpf(L);
#pragma unroll
                    for (int t = 0; t < 4; ++t) {
                        const float w0 = bitsf(__builtin_amdgcn_readlane(fbits(fw), 2 * t)), w1 = bitsf(__builtin_amdgcn_readlane(fbits(fw), 2 * t + 1));
                        wt[t] = hf ? w1 : w0;
                    }
                    have = true;
                }
                u32 bad = 0;
#pragma unroll
                for (int t = 0; t < 4; ++t) bad |= badg(go[t], tagA, tagB);
                if ((have && !__any(bad != 0)) || ++spins > SPIN_CAP) break;
            }
            const f32x4 ob0 = dec4(go[0]) * wt[0] + dec4(go[1]) * wt[1];
            const f32x4 ob1 = dec4(go[2]) * wt[2] + dec4(go[3]) * wt[3];
            float o[4];
#pragma unroll
            for (int k = 0; k < 4; ++k) o[k] = swap32_add(ob0[k], ob1[k]);
            *reinterpret_cast<u32x2*>(lds + L_O + lane * 8) = u32x2{pack_bf16(o[0], o[1]), pack_bf16(o[2], o[3])};
            asm volatile("" ::: "memory");
            if (lane == 0) __hip_atomic_store((LAS u32*)(lds + L_FLAG) + F_O, 1u, __ATOMIC_RELAXED, __HIP_MEMORY_SCOPE_WORKGROUP);
            asm volatile("" ::: "memory");
        }
        STAMP(5);
        const int rho = h / 6;
        if (h == rho * 6 && rho < 5) {
            const u32 off = WS_X2 + (u32)((j * 5 + rho) * 32) * 128 + lane * 16;
            u32x4 g0, g1, g2, g3;
            for (;;) {
                asm volatile("" ::: "memory");
                g0 = __builtin_amdgcn_raw_buffer_load_b128(wsr, off, 0, 16);
                g1 = __builtin_amdgcn_raw_buffer_load_b128(wsr, off + 1024, 0, 16);
                g2 = __builtin_amdgcn_raw_buffer_load_b128(wsr, off + 2048, 0, 16);
                g3 = __builtin_amdgcn_raw_buffer_load_b128(wsr, off + 3072, 0, 16);
                const u32 bad = badg(g0, tagA, tagB) | badg(g1, tagA, tagB) | badg(g2, tagA, tagB) | badg(g3, tagA, tagB);
                if (!__any(bad != 0)) break;
                if (++spins > SPIN_CAP) break;
            }
            STAMP(6);
            if (blockIdx.x == 0 && lane == 0) __hip_atomic_store(reinterpret_cast<u64*>(ws + WS_E), T, __ATOMIC_RELAXED, __HIP_MEMORY_SCOPE_AGENT);
            const f32x4 y4 = (dec4(g0) + dec4(g1)) + (dec4(g2) + dec4(g3));
            const float ya = swap32_add(y4[0], y4[1]), yb = swap32_add(y4[2], y4[3]);
            float y = swap16_add(ya, yb);
            y += dpp<ROR8>(y);
            if ((lane & 8) == 0) {
                const __bf16 ybf = (__bf16)y;
                out[hf * D + j * 80 + 16 * rho + 2 * (lane & 7) + ((lane >> 4) & 1)] = __builtin_bit_cast(unsigned short, ybf);
            }
        }
        STAMP(7);
    }
    if (wv < 4 || wv == 8) {
        const int nt = wv < 4 ? wv : 4;
        flag_wait(lds, F_WO, 4);
        const int q_ = (lane & 15) >> 2, p_ = lane & 3;
        const u32 tb = L_WO + (u32)(8 * r + q_) * 160 + 32 * nt + 8 * p_;
        s16x8 bf[4];
#pragma unroll
        for (int t = 0; t < 4; ++t) {
            const s16x4 b0 = __builtin_amdgcn_ds_read_tr16_b64_v4i16((LAS s16x4*)(lds + tb + t * 32 * 160));
            const s16x4 b1 = __builtin_amdgcn_ds_read_tr16_b64_v4i16((LAS s16x4*)(lds + tb + t * 32 * 160 + 4 * 160));
            bf[t] = s16x8{b0[0], b0[1], b0[2], b0[3], b1[0], b1[1], b1[2], b1[3]};
        }
        u32x2 tg;
        asm volatile("" : "+v"(bf[0]), "+v"(bf[1]), "+v"(bf[2]), "+v"(bf[3]));
        const u32 ab = (c < 2 ? L_O + c * 256 : L_Z) + 16 * r;
        f32x4 dacc = {0.f, 0.f, 0.f, 0.f}, dacc2 = {0.f, 0.f, 0.f, 0.f};
        u32x4 af[4];
#pragma nounroll
        for (u32 it = 0; it < (1u << 24); ++it) {
            asm volatile("" ::: "memory");
            const u32 fl = __hip_atomic_load((LAS u32*)(lds + L_FLAG) + F_O, __ATOMIC_RELAXED, __HIP_MEMORY_SCOPE_WORKGROUP);
            asm volatile("" ::: "memory");
#pragma unroll
            for (int t = 0; t < 4; ++t) af[t] = *reinterpret_cast<const u32x4*>(lds + ab + 64 * t);
            tg = *reinterpret_cast<const u32x2*>(lds + L_TAG);
            asm volatile("" : "+v"(af[0]), "+v"(af[1]), "+v"(af[2]), "+v"(af[3]), "+v"(tg) :: "memory");
            if (__builtin_amdgcn_readfirstlane(fl) != 0) break;
            __builtin_amdgcn_s_sleep(1);
        }
        STAMP(6);
        dacc = __builtin_amdgcn_mfma_f32_16x16x32_bf16(__builtin_bit_cast(bf16x8, af[0]), __builtin_bit_cast(bf16x8, bf[0]), dacc, 0, 0, 0);
        dacc2 = __builtin_amdgcn_mfma_f32_16x16x32_bf16(__builtin_bit_cast(bf16x8, af[1]), __builtin_bit_cast(bf16x8, bf[1]), dacc2, 0, 0, 0);
        dacc = __builtin_amdgcn_mfma_f32_16x16x32_bf16(__builtin_bit_cast(bf16x8, af[2]), __builtin_bit_cast(bf16x8, bf[2]), dacc, 0, 0, 0);
        dacc2 = __builtin_amdgcn_mfma_f32_16x16x32_bf16(__builtin_bit_cast(bf16x8, af[3]), __builtin_bit_cast(bf16x8, bf[3]), dacc2, 0, 0, 0);
        dacc += dacc2;
        const float n0 = dpp<XOR1>(dacc[0]), n1 = dpp<XOR1>(dacc[1]);
        if (lane < 16 && (lane & 1) == 0) {
            __builtin_amdgcn_raw_buffer_store_b128(enc4(dacc[0], dacc[1], n0, n1, tg[0], tg[1]), wsr,
                                                   WS_X2 + (u32)((j * 5 + nt) * 32 + h) * 128 + (lane >> 1) * 16, 0, 16);
        }
        STAMP(7);
    }
}

extern "C" void kernel_launch(void* const* d_in, const int* in_sizes, int n_in,
                              void* d_out, int out_size, void* d_ws, size_t ws_size,
                              hipStream_t stream) {
    fused_decode<<<256, NTHR, 0, stream>>>((const unsigned short*)d_in[0], (const u32x4*)d_in[1], (const u32x4*)d_in[2],
                                           (const u32x4*)d_in[3], (const u32x4*)d_in[4], (unsigned short*)d_out,
                                           (unsigned char*)d_ws);
}
```
